```python
import torch

def _softmax(x, axis=-1):
    return torch.softmax(x, dim=axis)
def _gelu(x):
    return torch.nn.functional.gelu(x, approximate="tanh")
def _silu(x):
    return torch.nn.functional.silu(x)
def _sigmoid(x):
    return torch.sigmoid(x)
def _layernorm(x, axis=-1, eps=1e-5):
    m = x.mean(dim=axis, keepdim=True)
    v = x.var(dim=axis, keepdim=True, unbiased=False)
    return (x - m) / torch.sqrt(v + eps)
def _scatter_add(tgt, idx, src, axis=0):
    return tgt.clone().index_add_(axis, idx, src)
def _segment_sum(src, seg, n):
    out = torch.zeros((n,) + src.shape[1:], dtype=src.dtype)
    return out.index_add_(0, seg, src)
def _pad(x, pw):
    flat = tuple(v for pair in reversed(pw) for v in pair)
    return torch.nn.functional.pad(x, flat)

kernel_name = 'standalone__kv_cache_quant_int4_int2_kv_with_per_channel_or_per_token_scales__decode'

def setup_inputs():

    _w = (lambda a: (lambda t: t.to(torch.bfloat16) if t.is_floating_point() else t)(torch.as_tensor(a)))
    B = 1
    Sq = 1
    Skv = 32768
    Hq = 32
    Hkv = 8
    Dh = 128
    bits = 4
    G = -1
    if G <= 0 or G >= Skv:
        G = max(1, Skv // 2)
    Skv = (Skv // G) * G
    ngk = Skv // G
    qmax = (1 << bits) - 1
    return {
        "q": _w((lambda *s: torch.randn(*s, dtype=torch.bfloat16) * 1.6)(B, Hq, Sq, Dh)),
        "qk": _w(torch.randint(0, qmax + 1, (B, Hkv, Skv, Dh)).to(torch.int32)),
        "qv": _w(torch.randint(0, qmax + 1, (B, Hkv, Skv, Dh)).to(torch.int32)),
        "k_scales": _w(torch.abs((lambda *s: torch.randn(*s, dtype=torch.bfloat16) * 1.6)(B, Hkv, ngk, Dh)) * 0.05 + 0.02),
        "k_zeros": _w((lambda *s: torch.randn(*s, dtype=torch.bfloat16) * 1.6)(B, Hkv, ngk, Dh) + float(qmax) / 2.0),
        "v_scales": _w(torch.abs((lambda *s: torch.randn(*s, dtype=torch.bfloat16) * 0.05)(B, Hkv, Skv, 1)) * 0.05 + 0.02),
        "v_zeros": _w((lambda *s: torch.randn(*s, dtype=torch.bfloat16) * 0.05)(B, Hkv, Skv, 1) + float(qmax) / 2.0),
    }

def reference(q, qk, qv, k_scales, k_zeros, v_scales, v_zeros):
    B, Hq, Sq, Dh = q.shape
    Hkv, Skv = qk.shape[1], qk.shape[2]
    ngk = k_scales.shape[2]
    G = Skv // ngk
    ks = (lambda x, n, axis: torch.repeat_interleave(x, n, dim=axis))(k_scales, G, axis=2)
    kz = (lambda x, n, axis: torch.repeat_interleave(x, n, dim=axis))(k_zeros, G, axis=2)
    k = ((lambda x, dt: x.to(dt))(qk, q.dtype) - kz) * ks
    v = ((lambda x, dt: x.to(dt))(qv, q.dtype) - v_zeros) * v_scales
    rep = Hq // Hkv
    if rep > 1:
        k = (lambda x, n, axis: torch.repeat_interleave(x, n, dim=axis))(k, rep, axis=1)
        v = (lambda x, n, axis: torch.repeat_interleave(x, n, dim=axis))(v, rep, axis=1)
    scale = float(Dh) ** -0.5
    logits = torch.einsum("bhqd,bhkd->bhqk", q, k) * scale
    p = _softmax(logits, axis=-1)
    o = torch.einsum("bhqk,bhkd->bhqd", p, v)
    return o
```

```cpp
#include <hip/hip_runtime.h>
#include <hip/hip_bf16.h>
#include <stdint.h>

typedef unsigned u32;
typedef u32 u32x4 __attribute__((ext_vector_type(4)));
typedef u32 u32x2 __attribute__((ext_vector_type(2)));
typedef float f32x4 __attribute__((ext_vector_type(4)));
typedef _Float16 f16x8 __attribute__((ext_vector_type(8)));
typedef _Float16 f16x2 __attribute__((ext_vector_type(2)));

constexpr int HQ = 32, HKV = 8, SKV = 32768, DH = 128, NGK = 2, GSZ = SKV / NGK;
constexpr int NSPLIT = 32;
constexpr int WAVES = 8;
constexpr int TOK_WG = SKV / NSPLIT;
#define WIN 32
#define AUX 2

__device__ __forceinline__ float bf_lo(u32 x) { return __builtin_bit_cast(float, x << 16); }
__device__ __forceinline__ float bf_hi(u32 x) { return __builtin_bit_cast(float, x & 0xffff0000u); }
__device__ __forceinline__ u32 pk(u32 a, u32 b) { return a | (b << 16); }
template <int CTRL> __device__ __forceinline__ float dpp(float x) {
  return __builtin_bit_cast(float, __builtin_amdgcn_mov_dpp(__builtin_bit_cast(int, x), CTRL, 0xf, 0xf, true));
}
constexpr int XOR1 = 0xB1, XOR2 = 0x4E;
__device__ __forceinline__ float head_max(float x) {
  x = fmaxf(x, dpp<XOR1>(x)); x = fmaxf(x, dpp<XOR2>(x));
  auto s = __builtin_amdgcn_permlane16_swap(__float_as_uint(x), __float_as_uint(x), false, false);
  x = fmaxf(__uint_as_float(s[0]), __uint_as_float(s[1]));
  auto t = __builtin_amdgcn_permlane32_swap(__float_as_uint(x), __float_as_uint(x), false, false);
  return fmaxf(__uint_as_float(t[0]), __uint_as_float(t[1]));
}
__device__ __forceinline__ float head_sum(float x) {
  x += dpp<XOR1>(x); x += dpp<XOR2>(x);
  auto s = __builtin_amdgcn_permlane16_swap(__float_as_uint(x), __float_as_uint(x), false, false);
  x = __uint_as_float(s[0]) + __uint_as_float(s[1]);
  auto t = __builtin_amdgcn_permlane32_swap(__float_as_uint(x), __float_as_uint(x), false, false);
  return __uint_as_float(t[0]) + __uint_as_float(t[1]);
}
__device__ __forceinline__ u32 pkh(float a, float b) {
  f16x2 h = {(_Float16)a, (_Float16)b};
  return __builtin_bit_cast(u32, h);
}
__device__ __forceinline__ float h_lo(u32 x) { return (float)__builtin_bit_cast(f16x2, x)[0]; }
__device__ __forceinline__ float h_hi(u32 x) { return (float)__builtin_bit_cast(f16x2, x)[1]; }
__device__ __forceinline__ f32x4 mfma(u32x4 a, u32x4 b, f32x4 c) {
  return __builtin_amdgcn_mfma_f32_16x16x32_f16(__builtin_bit_cast(f16x8, a), __builtin_bit_cast(f16x8, b), c, 0, 0, 0);
}

__global__ void __launch_bounds__(WAVES * 64, 2)
kivi_split(const uint16_t* __restrict__ q, const int* __restrict__ qk, const int* __restrict__ qv,
           const uint16_t* __restrict__ ks, const uint16_t* __restrict__ kz,
           const uint16_t* __restrict__ vs, const uint16_t* __restrict__ vz,
           float* __restrict__ po, float* __restrict__ pml) {
  __shared__ __attribute__((aligned(16))) float s_acc[WAVES * 4 * DH];
  __shared__ __attribute__((aligned(16))) float s_rec[WAVES * 4 * 4];
  const int b = blockIdx.x, h = b >> 5, s = b & 31, grp = s >> 4;
  const int tid = threadIdx.x, lane = tid & 63, w = __builtin_amdgcn_readfirstlane(tid >> 6);
  const int g = lane >> 4, r = lane & 15, hh = r >> 2, pi = r & 3;

  const int d0 = 16 * g + 4 * pi;
  const uint16_t* qp = q + (h * 4 + hh) * DH + d0;
  const uint16_t* ksp = ks + (h * NGK + grp) * DH + d0;
  const uint16_t* kzp = kz + (h * NGK + grp) * DH + d0;
  const u32x2 q0 = *(const u32x2*)qp, q1 = *(const u32x2*)(qp + 64);
  const u32x2 s0 = *(const u32x2*)ksp, s1 = *(const u32x2*)(ksp + 64);
  const u32x2 z0 = *(const u32x2*)kzp, z1 = *(const u32x2*)(kzp + 64);
  const int tok0 = h * SKV + s * TOK_WG;
  const int mytok = tok0 + (pi * WAVES + w) * 32 + 8 * g;
  const u32x4 vsv = *(const u32x4*)(vs + mytok);
  const u32x4 vzv = *(const u32x4*)(vz + mytok);

  const __amdgpu_buffer_rsrc_t rk = __builtin_amdgcn_make_buffer_rsrc((void*)qk, 0, HKV * SKV * DH * 4, 0x00020000);
  const __amdgpu_buffer_rsrc_t rv = __builtin_amdgcn_make_buffer_rsrc((void*)qv, 0, HKV * SKV * DH * 4, 0x00020000);
  const int koff = hh * 4096 + g * 64 + pi * 16;
  const int voff = g * 4096 + r * 16;
  const int wbase = (tok0 + w * 32) * 512;
  u32x4 R[WIN];
  auto issue = [&](int n) {
    if (n < 64) {
      const int T = n >> 4, j = (n & 15) >> 1, i = n & 1;
      R[n % WIN] = __builtin_amdgcn_raw_buffer_load_b128(rk, koff + j * 512 + i * 256, wbase + T * (WAVES * 32 * 512), AUX);
    } else if (n < 128) {
      const int m = n - 64, T = m >> 4, u = (m & 15) >> 2, hf = (m >> 1) & 1, e = m & 1;
      R[n % WIN] = __builtin_amdgcn_raw_buffer_load_b128(rv, voff + (2 * u + e) * 512 + hf * 256, wbase + T * (WAVES * 32 * 512), AUX);
    }
  };
#pragma unroll
  for (int n = 0; n < WIN; ++n) issue(n);
  __builtin_amdgcn_sched_barrier(0);

  float qq[8], zz[8];
  qq[0] = bf_lo(q0.x) * bf_lo(s0.x); qq[1] = bf_hi(q0.x) * bf_hi(s0.x); qq[2] = bf_lo(q0.y) * bf_lo(s0.y); qq[3] = bf_hi(q0.y) * bf_hi(s0.y);
  qq[4] = bf_lo(q1.x) * bf_lo(s1.x); qq[5] = bf_hi(q1.x) * bf_hi(s1.x); qq[6] = bf_lo(q1.y) * bf_lo(s1.y); qq[7] = bf_hi(q1.y) * bf_hi(s1.y);
  zz[0] = bf_lo(z0.x); zz[1] = bf_hi(z0.x); zz[2] = bf_lo(z0.y); zz[3] = bf_hi(z0.y);
  zz[4] = bf_lo(z1.x); zz[5] = bf_hi(z1.x); zz[6] = bf_lo(z1.y); zz[7] = bf_hi(z1.y);
  float qm = 0.f;
#pragma unroll
  for (int k = 0; k < 8; ++k) qm = fmaxf(qm, fabsf(qq[k]));
  qm = head_max(qm);
  const int eq = __builtin_amdgcn_frexp_expf(qm);
  u32x4 QB;
#pragma unroll
  for (int k = 0; k < 4; ++k) QB[k] = pkh(__builtin_amdgcn_ldexpf(qq[2 * k], 15 - eq), __builtin_amdgcn_ldexpf(qq[2 * k + 1], 15 - eq));
  float cq = 0.f;
#pragma unroll
  for (int k = 0; k < 4; ++k) { cq = fmaf(h_lo(QB[k]), zz[2 * k], cq); cq = fmaf(h_hi(QB[k]), zz[2 * k + 1], cq); }
  cq = head_sum(cq);
  constexpr float SC = 0.088388347648318440f * 1.4426950408889634f;
  const float c1 = __builtin_amdgcn_ldexpf(SC, eq + 9);
  const float cq2 = __builtin_amdgcn_ldexpf(cq, eq - 15) * SC;

  float keep[8];
#pragma unroll
  for (int T = 0; T < 4; ++T) {
#pragma unroll
    for (int j = 0; j < 8; ++j) {
      const int n = T * 16 + j * 2;
      const u32x4 x = R[n % WIN], y = R[(n + 1) % WIN];
      const u32x4 A = {pk(x.x, x.y), pk(x.z, x.w), pk(y.x, y.y), pk(y.z, y.w)};
      __builtin_amdgcn_sched_barrier(0);
      issue(n + WIN); issue(n + 1 + WIN);
      __builtin_amdgcn_sched_barrier(0);
      const f32x4 Dm = mfma(A, QB, f32x4{0.f, 0.f, 0.f, 0.f});
      const float S = (dpp<0x00>(Dm[0]) + dpp<0x55>(Dm[1])) + (dpp<0xAA>(Dm[2]) + dpp<0xFF>(Dm[3]));
      keep[j] = (T == 0 || pi == T) ? S : keep[j];
    }
  }
  float mr = keep[0];
#pragma unroll
  for (int j = 1; j < 8; ++j) mr = fmaxf(mr, keep[j]);
  mr = head_max(mr);
  float l = 0.f, wm = 0.f, wv[8];
#pragma unroll
  for (int j = 0; j < 8; ++j) {
    const float p = __builtin_amdgcn_exp2f((keep[j] - mr) * c1);
    l += p;
    wv[j] = p * ((j & 1) ? bf_hi(vsv[j >> 1]) : bf_lo(vsv[j >> 1]));
    wm = fmaxf(wm, fabsf(wv[j]));
  }
  wm = head_max(wm);
  const int ew = __builtin_amdgcn_frexp_expf(wm);
  u32x4 PW;
  float Z = 0.f;
#pragma unroll
  for (int k = 0; k < 4; ++k) {
    PW[k] = pkh(__builtin_amdgcn_ldexpf(wv[2 * k], 15 - ew), __builtin_amdgcn_ldexpf(wv[2 * k + 1], 15 - ew));
    Z = fmaf(h_lo(PW[k]), bf_lo(vzv[k]), Z); Z = fmaf(h_hi(PW[k]), bf_hi(vzv[k]), Z);
  }
  l = head_sum(l); Z = head_sum(Z);
  if (lane < 16 && pi == 0) {
    f32x4 rec = {fmaf(mr, c1, -cq2), l, __builtin_amdgcn_ldexpf(Z, ew - 15), __int_as_float(ew + 9)};
    *(f32x4*)(s_rec + (w * 4 + hh) * 4) = rec;
  }
  f32x4 acc[8];
#pragma unroll
  for (int k = 0; k < 8; ++k) acc[k] = f32x4{0.f, 0.f, 0.f, 0.f};
#pragma unroll
  for (int T = 0; T < 4; ++T) {
    u32x4 Bv[8];
    const u32x4 A = {pi == T ? PW[0] : 0u, pi == T ? PW[1] : 0u, pi == T ? PW[2] : 0u, pi == T ? PW[3] : 0u};
#pragma unroll
    for (int u = 0; u < 4; ++u)
#pragma unroll
      for (int hf = 0; hf < 2; ++hf) {
        const int n = 64 + T * 16 + u * 4 + hf * 2;
        const u32x4 x = R[n % WIN], y = R[(n + 1) % WIN];
#pragma unroll
        for (int i = 0; i < 4; ++i) Bv[hf * 4 + i][u] = pk(x[i], y[i]);
        __builtin_amdgcn_sched_barrier(0);
        issue(n + WIN); issue(n + 1 + WIN);
        __builtin_amdgcn_sched_barrier(0);
      }
#pragma unroll
    for (int k = 0; k < 8; ++k) acc[k] = mfma(A, Bv[k], acc[k]);
  }
  {
    f32x4 o0, o1;
#pragma unroll
    for (int i = 0; i < 4; ++i) {
      o0[i] = (acc[i][0] + acc[i][1]) + (acc[i][2] + acc[i][3]);
      o1[i] = (acc[4 + i][0] + acc[4 + i][1]) + (acc[4 + i][2] + acc[4 + i][3]);
    }
    float* dst = s_acc + (w * 4 + g) * DH + 4 * r;
    *(f32x4*)dst = o0; *(f32x4*)(dst + 64) = o1;
  }
  __syncthreads();
  {
    const int oh = tid >> 7, d = tid & 127;
    f32x4 rec[WAVES];
    float M = -INFINITY;
#pragma unroll
    for (int k = 0; k < WAVES; ++k) { rec[k] = *(const f32x4*)(s_rec + (k * 4 + oh) * 4); M = fmaxf(M, rec[k][0]); }
    float L = 0.f, O = 0.f;
#pragma unroll
    for (int k = 0; k < WAVES; ++k) {
      const float f = __builtin_amdgcn_exp2f(rec[k][0] - M);
      const float a = __builtin_amdgcn_ldexpf(s_acc[(k * 4 + oh) * DH + d], __float_as_int(rec[k][3])) - rec[k][2];
      L = fmaf(f, rec[k][1], L); O = fmaf(f, a, O);
    }
    const int slot = (h * 4 + oh) * NSPLIT + s;
    po[slot * DH + d] = O;
    if (d == 0) { pml[slot * 2] = M; pml[slot * 2 + 1] = L; }
  }
}

__global__ void __launch_bounds__(128) kivi_combine(const float* __restrict__ po, const float* __restrict__ pml, __hip_bfloat16* __restrict__ out) {
  const int hq = blockIdx.x, d = threadIdx.x;
  float o[NSPLIT]; float2 ml[NSPLIT];
#pragma unroll
  for (int s = 0; s < NSPLIT; ++s) { o[s] = po[(hq * NSPLIT + s) * DH + d]; ml[s] = *(const float2*)(pml + (hq * NSPLIT + s) * 2); }
  float M = -INFINITY;
#pragma unroll
  for (int s = 0; s < NSPLIT; ++s) M = fmaxf(M, ml[s].x);
  float L = 0.f, O = 0.f;
#pragma unroll
  for (int s = 0; s < NSPLIT; ++s) { const float f = __builtin_amdgcn_exp2f(ml[s].x - M); L = fmaf(f, ml[s].y, L); O = fmaf(f, o[s], O); }
  out[hq * DH + d] = __float2bfloat16(O / L);
}

extern "C" void kernel_launch(void* const* d_in, const int* in_sizes, int n_in, void* d_out, int out_size,
                              void* d_ws, size_t ws_size, hipStream_t stream) {
  float* po = (float*)d_ws;
  float* pml = po + HQ * NSPLIT * DH;
  kivi_split<<<HKV * NSPLIT, WAVES * 64, 0, stream>>>((const uint16_t*)d_in[0], (const int*)d_in[1], (const int*)d_in[2],
      (const uint16_t*)d_in[3], (const uint16_t*)d_in[4], (const uint16_t*)d_in[5], (const uint16_t*)d_in[6], po, pml);
  kivi_combine<<<HQ, 128, 0, stream>>>(po, pml, (__hip_bfloat16*)d_out);
}
```

```cpp
#define SMALL_POS 1
#include <hip/hip_runtime.h>
#include <hip/hip_bf16.h>
#include <stdint.h>

typedef unsigned u32;
typedef unsigned long long u64;
typedef u32 u32x4 __attribute__((ext_vector_type(4)));
typedef u32 u32x2 __attribute__((ext_vector_type(2)));
typedef float f32x4 __attribute__((ext_vector_type(4)));
typedef _Float16 f16x8 __attribute__((ext_vector_type(8)));
typedef _Float16 f16x2 __attribute__((ext_vector_type(2)));
typedef __bf16 bf16x2 __attribute__((ext_vector_type(2)));

constexpr int HQ = 32, HKV = 8, SKV = 32768, DH = 128, NGK = 2;
constexpr int NSPLIT = 32;
constexpr int WAVES = 4;
constexpr int TOK_WG = SKV / NSPLIT;
constexpr int TILE_STEP = WAVES * 32 * 512;
#define CW 0
#define POLL_SLEEP 1
#define ABL 0
#define XMAP 0

__device__ __forceinline__ float bf_lo(u32 x) { return __uint_as_float(x << 16); }
__device__ __forceinline__ float bf_hi(u32 x) { return __uint_as_float(x & 0xffff0000u); }
__device__ __forceinline__ u32 pk(u32 a, u32 b) { return a | (b << 16); }
template <int CTRL> __device__ __forceinline__ float dpp(float x) {
  return __int_as_float(__builtin_amdgcn_mov_dpp(__float_as_int(x), CTRL, 0xf, 0xf, true));
}
constexpr int XOR1 = 0xB1, XOR2 = 0x4E, ROR8 = 0x128;
__device__ __forceinline__ float rows_max(float x) {
  auto s = __builtin_amdgcn_permlane16_swap(__float_as_uint(x), __float_as_uint(x), false, false);
  x = fmaxf(__uint_as_float(s[0]), __uint_as_float(s[1]));
  auto t = __builtin_amdgcn_permlane32_swap(__float_as_uint(x), __float_as_uint(x), false, false);
  return fmaxf(__uint_as_float(t[0]), __uint_as_float(t[1]));
}
__device__ __forceinline__ float rows_sum(float x) {
  auto s = __builtin_amdgcn_permlane16_swap(__float_as_uint(x), __float_as_uint(x), false, false);
  x = __uint_as_float(s[0]) + __uint_as_float(s[1]);
  auto t = __builtin_amdgcn_permlane32_swap(__float_as_uint(x), __float_as_uint(x), false, false);
  return __uint_as_float(t[0]) + __uint_as_float(t[1]);
}
__device__ __forceinline__ float head_max(float x) { x = fmaxf(x, dpp<XOR1>(x)); x = fmaxf(x, dpp<XOR2>(x)); return rows_max(x); }
__device__ __forceinline__ float head_sum(float x) { x += dpp<XOR1>(x); x += dpp<XOR2>(x); return rows_sum(x); }
__device__ __forceinline__ float red8_max(float x) { return rows_max(fmaxf(x, dpp<ROR8>(x))); }
__device__ __forceinline__ float red8_sum(float x) { return rows_sum(x + dpp<ROR8>(x)); }
__device__ __forceinline__ u32 pkh(float a, float b) { f16x2 h = {(_Float16)a, (_Float16)b}; return __builtin_bit_cast(u32, h); }
__device__ __forceinline__ float h_lo(u32 x) { return (float)__builtin_bit_cast(f16x2, x)[0]; }
__device__ __forceinline__ float h_hi(u32 x) { return (float)__builtin_bit_cast(f16x2, x)[1]; }
__device__ __forceinline__ f32x4 mfma(u32x4 a, u32x4 b, f32x4 c) {
  return __builtin_amdgcn_mfma_f32_16x16x32_f16(__builtin_bit_cast(f16x8, a), __builtin_bit_cast(f16x8, b), c, 0, 0, 0);
}

#define TS(k) do {} while (0)
constexpr int WS_EP = 0, WS_ML = 4096, WS_DAT = WS_ML + HKV * 4 * NSPLIT * 16, WS_END = WS_DAT + HKV * NSPLIT * NSPLIT * 128;

#define LDS_(reg, voff, srd, soff, imm) asm volatile("buffer_load_dwordx4 %0, %1, %2, %3 offen offset:%4 nt" : "+v"(reg) : "v"(voff), "s"(srd), "s"(soff), "i"(imm) : "memory")
#define LDN_(reg, voff, srd, soff, imm) asm volatile("buffer_load_dwordx4 %0, %1, %2, %3 offen offset:%4 nt" : "=v"(reg) : "v"(voff), "s"(srd), "s"(soff), "i"(imm) : "memory")
#define WAIT2(a, b, n) asm volatile("s_waitcnt vmcnt(%2)" : "+v"(a), "+v"(b) : "i"(n) : "memory")
#define WAIT4(a, b, c, d, n) asm volatile("s_waitcnt vmcnt(%4)" : "+v"(a), "+v"(b), "+v"(c), "+v"(d) : "i"(n) : "memory")
#define Q_NT ""
#define ST_AUX 16
#define LD_AUX 16
#define GLD2(reg, voff, base, imm) asm volatile("global_load_dwordx2 %0, %1, %2 offset:%3" Q_NT : "=v"(reg) : "v"(voff), "s"(base), "i"(imm) : "memory")
#define SMALL_NT " nt"
#define GLD4(reg, voff, base, imm) asm volatile("global_load_dwordx4 %0, %1, %2 offset:%3" SMALL_NT : "=v"(reg) : "v"(voff), "s"(base), "i"(imm) : "memory")

__device__ __forceinline__ u32x4 make_srd(const void* p, u32 bytes) {
  const u64 a = (u64)p;
  return u32x4{(u32)a, (u32)(a >> 32) & 0xffffu, bytes, 0x00020000u};
}

__device__ __forceinline__ void k_keep(float& keep, const f32x4 Dm, u32 mine) {
  const float S = ((dpp<0x00>(Dm[0]) + dpp<0x55>(Dm[1])) + dpp<0xAA>(Dm[2])) + dpp<0xFF>(Dm[3]);
  keep = __uint_as_float((__float_as_uint(S) & mine) | (__float_as_uint(keep) & ~mine));
}
__device__ __forceinline__ void k_tile(u32x4 (&X)[16], const u32x4 QB, float (&keep)[8], u32 mine, u32 voff, u32x4 srd, u32 soff) {
  f32x4 Dp;
#pragma unroll
  for (int j = 0; j < 8; ++j) {
    WAIT2(X[2 * j], X[2 * j + 1], 30);
    const u32x4 x = X[2 * j], y = X[2 * j + 1];
    const u32x4 A = {pk(x.x, x.y), pk(x.z, x.w), pk(y.x, y.y), pk(y.z, y.w)};
    LDS_(X[2 * j], voff, srd, soff, (2 * j) * 256);
    LDS_(X[2 * j + 1], voff, srd, soff, (2 * j + 1) * 256);
    if (ABL & 4) { keep[j] = __uint_as_float(__float_as_uint(keep[j]) ^ ((A.x ^ A.y ^ A.z ^ A.w) & 0xff)); continue; }
    const f32x4 Dm = mfma(A, QB, f32x4{0.f, 0.f, 0.f, 0.f});
    __builtin_amdgcn_sched_barrier(0);
    if (j > 0) k_keep(keep[j - 1], Dp, mine);
    __builtin_amdgcn_sched_barrier(0);
    Dp = Dm;
  }
  if (!(ABL & 4)) k_keep(keep[7], Dp, mine);
}
template <int LEFT>
__device__ __forceinline__ void v_tile(u32x4 (&X)[16], const u32x4 PW, u32 mine, f32x4 (&acc)[8], u32 voff, u32x4 srd, u32 soff) {
  u32x4 Bv[8];
  const u32x4 A = {PW.x & mine, PW.y & mine, PW.z & mine, PW.w & mine};
#pragma unroll
  for (int u = 0; u < 4; ++u) {
    WAIT4(X[4 * u], X[4 * u + 1], X[4 * u + 2], X[4 * u + 3], LEFT < 0 ? 28 : LEFT + 12 - 4 * u);
#pragma unroll
    for (int hf = 0; hf < 2; ++hf)
#pragma unroll
      for (int i = 0; i < 4; ++i) Bv[hf * 4 + i][u] = pk(X[4 * u + hf][i], X[4 * u + 2 + hf][i]);
    if (LEFT < 0) {
#pragma unroll
      for (int n = 4 * u; n < 4 * u + 4; ++n) LDS_(X[n], voff, srd, soff, n * 256);
    }
    __builtin_amdgcn_sched_barrier(0);
  }
#pragma unroll
  for (int k = 0; k < 8; ++k) { if (ABL & 8) { asm volatile("" :: "v"(Bv[k]), "v"(A)); } else acc[k] = mfma(A, Bv[k], acc[k]); }
  __builtin_amdgcn_sched_barrier(0);
}

__global__ void __launch_bounds__(WAVES * 64, 2)
kivi_decode(const uint16_t* __restrict__ q, const int* __restrict__ qk, const int* __restrict__ qv,
            const uint16_t* __restrict__ ks, const uint16_t* __restrict__ kz,
            const uint16_t* __restrict__ vs, const uint16_t* __restrict__ vz,
            unsigned char* ws, u32* __restrict__ out) {
  TS(0);
  __shared__ __attribute__((aligned(16))) float s_acc[WAVES * 4 * DH];
  __shared__ __attribute__((aligned(16))) float s_rec[WAVES * 4 * 4];
  const int b = blockIdx.x, h = XMAP ? (b & 7) : (b >> 5), s = XMAP ? (b >> 3) : (b & 31), grp = s >> 4;
  const int tid = threadIdx.x, lane = tid & 63, w = __builtin_amdgcn_readfirstlane(tid >> 6);
  const int g = lane >> 4, r = lane & 15, hh = r >> 2, pi = r & 3;
  const int tok0 = h * SKV + s * TOK_WG;
  const u32x4 rk = make_srd(qk, HKV * SKV * DH * 4), rv = make_srd(qv, HKV * SKV * DH * 4);
  const u32 koff = hh * 4096 + g * 64 + pi * 16;
  const u32 voff = g * 4096 + r * 16;
  const u32 wbase = (u32)(tok0 + w * 32) * 512u;
  u32x4 XA[16], XB[16];
  u32x2 q0, q1, s0, s1, z0, z1;
  u64 ep;
  u32x4 vsA, vsB, vzA, vzB;
  u64* epp = (u64*)(ws + WS_EP + h * 256);
  asm volatile("s_load_dwordx2 %0, %1, 0x0" : "=s"(ep) : "s"(epp) : "memory");
  auto small = [&]() {
    const u32 d0 = (16 * g + 4 * pi) * 2;
    const uint16_t* qb = q + h * 4 * DH; const uint16_t* ksb = ks + (h * NGK + grp) * DH; const uint16_t* kzb = kz + (h * NGK + grp) * DH;
    const u32 qo = hh * (DH * 2) + d0;
    if (ABL & 32) { q0 = q1 = s0 = s1 = z0 = z1 = u32x2{0x3f803f80u + lane, 0x3f803f80u}; } else {
    GLD2(q0, qo, qb, 0); GLD2(q1, qo, qb, 128); GLD2(s0, d0, ksb, 0); GLD2(s1, d0, ksb, 128); GLD2(z0, d0, kzb, 0); GLD2(z1, d0, kzb, 128); }
    const u32 to = (u32)(pi * 2 * WAVES * 32 + 8 * g) * 2u;
    const uint16_t* vsb = vs + tok0 + w * 32; const uint16_t* vzb = vz + tok0 + w * 32;
    if (ABL & 16) { vsA = vsB = vzA = vzB = u32x4{0x3f803f80u, 0x3f803f80u + lane, 0x3f803f80u, 0x3f803f80u}; } else {
    GLD4(vsA, to, vsb, 0); GLD4(vsB, to, vsb, WAVES * 32 * 2); GLD4(vzA, to, vzb, 0); GLD4(vzB, to, vzb, WAVES * 32 * 2); }
  };
  if (ABL & 2) { q0 = q1 = s0 = s1 = z0 = z1 = u32x2{0x3f803f80u + lane, 0x3f803f80u}; vsA = vsB = vzA = vzB = u32x4{0x3f803f80u, 0x3f803f80u + lane, 0x3f803f80u, 0x3f803f80u}; }
  if (SMALL_POS == 0 && !(ABL & 2)) small();
#pragma unroll
  for (int n = 0; n < 16; ++n) LDN_(XA[n], koff, rk, wbase, n * 256);
  if (SMALL_POS == 1 && !(ABL & 2)) small();
#pragma unroll
  for (int n = 0; n < 16; ++n) LDN_(XB[n], koff, rk, wbase + TILE_STEP, n * 256);
  asm volatile("s_waitcnt vmcnt(%11) lgkmcnt(0)" : "+v"(q0), "+v"(q1), "+v"(s0), "+v"(s1), "+v"(z0), "+v"(z1), "+v"(vsA), "+v"(vsB), "+v"(vzA), "+v"(vzB), "+s"(ep)
               : "i"(SMALL_POS == 0 || (ABL & 2) ? 32 : 16) : "memory");

  TS(1);
  float qq[8], zz[8];
  qq[0] = bf_lo(q0.x) * bf_lo(s0.x); qq[1] = bf_hi(q0.x) * bf_hi(s0.x); qq[2] = bf_lo(q0.y) * bf_lo(s0.y); qq[3] = bf_hi(q0.y) * bf_hi(s0.y);
  qq[4] = bf_lo(q1.x) * bf_lo(s1.x); qq[5] = bf_hi(q1.x) * bf_hi(s1.x); qq[6] = bf_lo(q1.y) * bf_lo(s1.y); qq[7] = bf_hi(q1.y) * bf_hi(s1.y);
  zz[0] = bf_lo(z0.x); zz[1] = bf_hi(z0.x); zz[2] = bf_lo(z0.y); zz[3] = bf_hi(z0.y);
  zz[4] = bf_lo(z1.x); zz[5] = bf_hi(z1.x); zz[6] = bf_lo(z1.y); zz[7] = bf_hi(z1.y);
  float qm = 0.f;
#pragma unroll
  for (int k = 0; k < 8; ++k) qm = fmaxf(qm, fabsf(qq[k]));
  qm = head_max(qm);
  const int eq = __builtin_amdgcn_frexp_expf(qm);
  u32x4 QB;
#pragma unroll
  for (int k = 0; k < 4; ++k) QB[k] = pkh(__builtin_amdgcn_ldexpf(qq[2 * k], 15 - eq), __builtin_amdgcn_ldexpf(qq[2 * k + 1], 15 - eq));
  float cq = 0.f;
#pragma unroll
  for (int k = 0; k < 4; ++k) { cq = fmaf(h_lo(QB[k]), zz[2 * k], cq); cq = fmaf(h_hi(QB[k]), zz[2 * k + 1], cq); }
  cq = head_sum(cq);
  constexpr float SC = 0.088388347648318440f * 1.4426950408889634f;
  const float c1 = __builtin_amdgcn_ldexpf(SC, eq + 9);
  const float cq2 = __builtin_amdgcn_ldexpf(cq, eq - 15) * SC;

  float kA[8], kB[8];
#pragma unroll
  for (int j = 0; j < 8; ++j) { kA[j] = 0.f; kB[j] = 0.f; }
#pragma unroll 1
  for (int i = 0; i < 4; ++i) {
    const bool lastk = i == 3;
    const u32x4 srd = lastk ? rv : rk;
    const u32 vo = lastk ? voff : koff;
    const u32 so = wbase + (lastk ? 0 : (2 * i + 2) * TILE_STEP);
    const u32 mine = pi == i ? ~0u : 0u;
    k_tile(XA, QB, kA, mine, vo, srd, so);
    k_tile(XB, QB, kB, mine, vo, srd, so + TILE_STEP);
  }
  TS(2);
  float mr = fmaxf(kA[0], kB[0]);
#pragma unroll
  for (int j = 1; j < 8; ++j) mr = fmaxf(mr, fmaxf(kA[j], kB[j]));
  mr = head_max(mr);
  float l = 0.f, wm = 0.f;
#pragma unroll
  for (int j = 0; j < 8; ++j) {
    const float pa = __builtin_amdgcn_exp2f((kA[j] - mr) * c1), pb = __builtin_amdgcn_exp2f((kB[j] - mr) * c1);
    l += pa + pb;
    kA[j] = pa * ((j & 1) ? bf_hi(vsA[j >> 1]) : bf_lo(vsA[j >> 1]));
    kB[j] = pb * ((j & 1) ? bf_hi(vsB[j >> 1]) : bf_lo(vsB[j >> 1]));
    wm = fmaxf(wm, fmaxf(fabsf(kA[j]), fabsf(kB[j])));
  }
  wm = head_max(wm);
  const int ew = __builtin_amdgcn_frexp_expf(wm);
  u32x4 PA, PB;
  float Z = 0.f;
#pragma unroll
  for (int k = 0; k < 4; ++k) {
    PA[k] = pkh(__builtin_amdgcn_ldexpf(kA[2 * k], 15 - ew), __builtin_amdgcn_ldexpf(kA[2 * k + 1], 15 - ew));
    PB[k] = pkh(__builtin_amdgcn_ldexpf(kB[2 * k], 15 - ew), __builtin_amdgcn_ldexpf(kB[2 * k + 1], 15 - ew));
    Z = fmaf(h_lo(PA[k]), bf_lo(vzA[k]), Z); Z = fmaf(h_hi(PA[k]), bf_hi(vzA[k]), Z);
    Z = fmaf(h_lo(PB[k]), bf_lo(vzB[k]), Z); Z = fmaf(h_hi(PB[k]), bf_hi(vzB[k]), Z);
  }
  l = head_sum(l); Z = head_sum(Z);
  if (lane < 16 && pi == 0) {
    f32x4 rec = {fmaf(mr, c1, -cq2), l, __builtin_amdgcn_ldexpf(Z, ew - 15), __int_as_float(ew + 9)};
    *(f32x4*)(s_rec + (w * 4 + hh) * 4) = rec;
  }
  f32x4 acc[8];
#pragma unroll
  for (int k = 0; k < 8; ++k) acc[k] = f32x4{0.f, 0.f, 0.f, 0.f};
#pragma unroll 1
  for (int i = 0; i < 3; ++i) {
    const u32 so = wbase + (2 * i + 2) * TILE_STEP;
    const u32 mine = pi == i ? ~0u : 0u;
    v_tile<-1>(XA, PA, mine, acc, voff, rv, so);
    v_tile<-1>(XB, PB, mine, acc, voff, rv, so + TILE_STEP);
  }
  v_tile<16>(XA, PA, pi == 3 ? ~0u : 0u, acc, voff, rv, 0);
  v_tile<0>(XB, PB, pi == 3 ? ~0u : 0u, acc, voff, rv, 0);
  TS(3);
  {
    f32x4 o0, o1;
#pragma unroll
    for (int i = 0; i < 4; ++i) {
      o0[i] = (acc[i][0] + acc[i][1]) + (acc[i][2] + acc[i][3]);
      o1[i] = (acc[4 + i][0] + acc[4 + i][1]) + (acc[4 + i][2] + acc[4 + i][3]);
    }
    float* dst = s_acc + (w * 4 + g) * DH + 4 * r;
    *(f32x4*)dst = o0; *(f32x4*)(dst + 64) = o1;
  }
  __syncthreads();
  const u32 tagA = (u32)ep + 1u, tagB = (u32)(ep >> 32) + 1u;
  const __amdgpu_buffer_rsrc_t rw = __builtin_amdgcn_make_buffer_rsrc((void*)ws, 0, WS_END, 0x00020000);
  {
    f32x4 rec[WAVES];
    float M = -INFINITY;
#pragma unroll
    for (int k = 0; k < WAVES; ++k) { rec[k] = *(const f32x4*)(s_rec + (k * 4 + w) * 4); M = fmaxf(M, rec[k][0]); }
    float L = 0.f, O0 = 0.f, O1 = 0.f;
#pragma unroll
    for (int k = 0; k < WAVES; ++k) {
      const float f = __builtin_amdgcn_exp2f(rec[k][0] - M);
      const float2 a = *(const float2*)(s_acc + (k * 4 + w) * DH + 2 * lane);
      const int e = __float_as_int(rec[k][3]);
      L = fmaf(f, rec[k][1], L);
      O0 = fmaf(f, __builtin_amdgcn_ldexpf(a.x, e) - rec[k][2], O0);
      O1 = fmaf(f, __builtin_amdgcn_ldexpf(a.y, e) - rec[k][2], O1);
    }
    const u32x4 gd = {__float_as_uint(O0), tagA, __float_as_uint(O1), tagB};
    __builtin_amdgcn_raw_buffer_store_b128(gd, rw, WS_DAT + ((h * NSPLIT + w * 8 + (lane >> 3)) * NSPLIT + s) * 128 + (lane & 7) * 16, 0, ST_AUX);
    if (lane == 0) {
      const u32x4 gm = {__float_as_uint(M), tagA, __float_as_uint(L), tagB};
      __builtin_amdgcn_raw_buffer_store_b128(gm, rw, WS_ML + ((h * 4 + w) * NSPLIT + s) * 16, 0, ST_AUX);
    }
  }
  TS(4);
  if (!(ABL & 1) && w == CW) {
    const int pl = lane >> 3, gi = lane & 7, oh = s >> 3;
    const int doff = WS_DAT + ((h * NSPLIT + s) * NSPLIT + pl) * 128 + gi * 16, moff = WS_ML + ((h * 4 + oh) * NSPLIT + pl) * 16;
    u32x4 gd[4], gm[4];
    bool ok[4] = {false, false, false, false};
    const u64 tstart = __builtin_amdgcn_s_memrealtime();
    for (;;) {
#pragma unroll
      for (int i = 0; i < 4; ++i)
        if (!ok[i]) {
          gd[i] = __builtin_amdgcn_raw_buffer_load_b128(rw, doff + i * 1024, 0, LD_AUX);
          gm[i] = __builtin_amdgcn_raw_buffer_load_b128(rw, moff + i * 128, 0, LD_AUX);
        }
      bool all = true;
#pragma unroll
      for (int i = 0; i < 4; ++i) { ok[i] = (gd[i].y == tagA) & (gd[i].w == tagB) & (gm[i].y == tagA) & (gm[i].w == tagB); all = all & ok[i]; }
      if (__all(all)) break;
      if (__builtin_amdgcn_s_memrealtime() - tstart > 20000000ull) break;
      __builtin_amdgcn_s_sleep(POLL_SLEEP);
      asm volatile("" ::: "memory");
    }
    TS(5);
    float M = -INFINITY;
#pragma unroll
    for (int i = 0; i < 4; ++i) M = fmaxf(M, __uint_as_float(gm[i].x));
    M = red8_max(M);
    float L = 0.f, O0 = 0.f, O1 = 0.f;
#pragma unroll
    for (int i = 0; i < 4; ++i) {
      const float f = __builtin_amdgcn_exp2f(__uint_as_float(gm[i].x) - M);
      L = fmaf(f, __uint_as_float(gm[i].z), L); O0 = fmaf(f, __uint_as_float(gd[i].x), O0); O1 = fmaf(f, __uint_as_float(gd[i].z), O1);
    }
    L = red8_sum(L); O0 = red8_sum(O0); O1 = red8_sum(O1);
    if (lane < 8) {
      const float inv = 1.f / L;
      const bf16x2 o = {(__bf16)(O0 * inv), (__bf16)(O1 * inv)};
      out[((h * 4 + oh) * DH + (s & 7) * 16) / 2 + lane] = __builtin_bit_cast(u32, o);
    }
    if (s == 0 && lane == 0) __hip_atomic_store(epp, ((u64)tagB << 32) | tagA, __ATOMIC_RELAXED, __HIP_MEMORY_SCOPE_AGENT);
  }
}

extern "C" void kernel_launch(void* const* d_in, const int* in_sizes, int n_in, void* d_out, int out_size,
                              void* d_ws, size_t ws_size, hipStream_t stream) {
  kivi_decode<<<HKV * NSPLIT, WAVES * 64, 0, stream>>>((const uint16_t*)d_in[0], (const int*)d_in[1], (const int*)d_in[2],
      (const uint16_t*)d_in[3], (const uint16_t*)d_in[4], (const uint16_t*)d_in[5], (const uint16_t*)d_in[6], (unsigned char*)d_ws, (u32*)d_out);
}
```

```cpp
#include <hip/hip_runtime.h>
#include <hip/hip_bf16.h>
#include <stdint.h>

typedef unsigned u32;
typedef unsigned long long u64;
typedef u32 u32x4 __attribute__((ext_vector_type(4)));
typedef u32 u32x2 __attribute__((ext_vector_type(2)));
typedef float f32x4 __attribute__((ext_vector_type(4)));
typedef _Float16 f16x8 __attribute__((ext_vector_type(8)));
typedef _Float16 f16x2 __attribute__((ext_vector_type(2)));
typedef __bf16 bf16x2 __attribute__((ext_vector_type(2)));

constexpr int HQ = 32, HKV = 8, SKV = 32768, DH = 128, NGK = 2;
constexpr int NSPLIT = 32;
constexpr int WAVES = 4;
constexpr int TOK_WG = SKV / NSPLIT;
constexpr int TILE_STEP = WAVES * 32 * 512;
#define HPRIO 3
#define LATEF 1
#define MWAIT 2800
#define MSLEEP 32
#define POLL_SLEEP 0
#define PACE_T 3750
#define PACE_PRO 1
#define PACE_TO 3550
#define PACE_A 12
#define SMALL_POS 0

__device__ __forceinline__ float bf_lo(u32 x) { return __uint_as_float(x << 16); }
__device__ __forceinline__ float bf_hi(u32 x) { return __uint_as_float(x & 0xffff0000u); }
__device__ __forceinline__ u32 pk(u32 a, u32 b) { return a | (b << 16); }
template <int CTRL> __device__ __forceinline__ float dpp(float x) {
  return __int_as_float(__builtin_amdgcn_mov_dpp(__float_as_int(x), CTRL, 0xf, 0xf, true));
}
constexpr int XOR1 = 0xB1, XOR2 = 0x4E, ROR8 = 0x128;
__device__ __forceinline__ float rows_max(float x) {
  auto s = __builtin_amdgcn_permlane16_swap(__float_as_uint(x), __float_as_uint(x), false, false);
  x = fmaxf(__uint_as_float(s[0]), __uint_as_float(s[1]));
  auto t = __builtin_amdgcn_permlane32_swap(__float_as_uint(x), __float_as_uint(x), false, false);
  return fmaxf(__uint_as_float(t[0]), __uint_as_float(t[1]));
}
__device__ __forceinline__ float rows_sum(float x) {
  auto s = __builtin_amdgcn_permlane16_swap(__float_as_uint(x), __float_as_uint(x), false, false);
  x = __uint_as_float(s[0]) + __uint_as_float(s[1]);
  auto t = __builtin_amdgcn_permlane32_swap(__float_as_uint(x), __float_as_uint(x), false, false);
  return __uint_as_float(t[0]) + __uint_as_float(t[1]);
}
__device__ __forceinline__ float head_max(float x) { x = fmaxf(x, dpp<XOR1>(x)); x = fmaxf(x, dpp<XOR2>(x)); return rows_max(x); }
__device__ __forceinline__ float head_sum(float x) { x += dpp<XOR1>(x); x += dpp<XOR2>(x); return rows_sum(x); }
__device__ __forceinline__ float red8_max(float x) { return rows_max(fmaxf(x, dpp<ROR8>(x))); }
__device__ __forceinline__ float red8_sum(float x) { return rows_sum(x + dpp<ROR8>(x)); }
__device__ __forceinline__ u32 pkh(float a, float b) { f16x2 h = {(_Float16)a, (_Float16)b}; return __builtin_bit_cast(u32, h); }
__device__ __forceinline__ float h_lo(u32 x) { return (float)__builtin_bit_cast(f16x2, x)[0]; }
__device__ __forceinline__ float h_hi(u32 x) { return (float)__builtin_bit_cast(f16x2, x)[1]; }
__device__ __forceinline__ f32x4 mfma(u32x4 a, u32x4 b, f32x4 c) {
  return __builtin_amdgcn_mfma_f32_16x16x32_f16(__builtin_bit_cast(f16x8, a), __builtin_bit_cast(f16x8, b), c, 0, 0, 0);
}

#define TS(k) do {} while (0)
constexpr int WS_EP = 0, WS_ML = 4096, WS_DAT = WS_ML + HKV * NSPLIT * 128, WS_END = WS_DAT + HKV * NSPLIT * NSPLIT * 128;

#define LDS_(reg, voff, srd, soff, imm) asm volatile("buffer_load_dwordx4 %0, %1, %2, %3 offen offset:%4 nt" : "+v"(reg) : "v"(voff), "s"(srd), "s"(soff), "i"(imm) : "memory")
#define LDN_(reg, voff, srd, soff, imm) asm volatile("buffer_load_dwordx4 %0, %1, %2, %3 offen offset:%4 nt" : "=v"(reg) : "v"(voff), "s"(srd), "s"(soff), "i"(imm) : "memory")
#define WAIT2(a, b, n) asm volatile("s_waitcnt vmcnt(%2)" : "+v"(a), "+v"(b) : "i"(n) : "memory")
#define WAIT4(a, b, c, d, n) asm volatile("s_waitcnt vmcnt(%4)" : "+v"(a), "+v"(b), "+v"(c), "+v"(d) : "i"(n) : "memory")
#define Q_NT ""
#define ST_AUX 16
#define LD_AUX 16
#define GLD2(reg, voff, base, imm) asm volatile("global_load_dwordx2 %0, %1, %2 offset:%3" Q_NT : "=v"(reg) : "v"(voff), "s"(base), "i"(imm) : "memory")
#define SMALL_NT " nt"
#define GLD4(reg, voff, base, imm) asm volatile("global_load_dwordx4 %0, %1, %2 offset:%3" SMALL_NT : "=v"(reg) : "v"(voff), "s"(base), "i"(imm) : "memory")

#define PACE(due) do { if (PACE_T) asm volatile("1:\n\ts_memrealtime vcc\n\ts_waitcnt lgkmcnt(0)\n\ts_sub_i32 vcc_lo, vcc_lo, %0\n\ts_cmp_ge_i32 vcc_lo, 0\n\ts_cbranch_scc1 2f\n\ts_sleep 1\n\ts_branch 1b\n2:" :: "s"(due) : "scc", "vcc", "memory"); } while (0)
#define DUE(t0, n) ((t0).x + (u32)((((int)(n) - PACE_A) * (int)(t0).y) >> 8))
__device__ __forceinline__ u32x4 make_srd(const void* p, u32 bytes) {
  const u64 a = (u64)p;
  return u32x4{(u32)a, (u32)(a >> 32) & 0xffffu, bytes, 0x00020000u};
}

__device__ __forceinline__ void k_keep(float& keep, const f32x4 Dm, u32 mine) {
  const float S = ((dpp<0x00>(Dm[0]) + dpp<0x55>(Dm[1])) + dpp<0xAA>(Dm[2])) + dpp<0xFF>(Dm[3]);
  keep = __uint_as_float((__float_as_uint(S) & mine) | (__float_as_uint(keep) & ~mine));
}
__device__ __forceinline__ void k_tile(u32x4 (&X)[16], const u32x4 QB, float (&keep)[8], u32 mine, u32 voff, u32x4 srd, u32 soff, u32x2 t0, int n0) {
  f32x4 Dp;
#pragma unroll
  for (int j = 0; j < 8; ++j) {
    WAIT2(X[2 * j], X[2 * j + 1], 30);
    const u32x4 x = X[2 * j], y = X[2 * j + 1];
    const u32x4 A = {pk(x.x, x.y), pk(x.z, x.w), pk(y.x, y.y), pk(y.z, y.w)};
    if ((j & 1) == 0) PACE(DUE(t0, n0 + 2 * j));
    LDS_(X[2 * j], voff, srd, soff, (2 * j) * 256);
    LDS_(X[2 * j + 1], voff, srd, soff, (2 * j + 1) * 256);
    const f32x4 Dm = mfma(A, QB, f32x4{0.f, 0.f, 0.f, 0.f});
    __builtin_amdgcn_sched_barrier(0);
    if (j > 0) k_keep(keep[j - 1], Dp, mine);
    __builtin_amdgcn_sched_barrier(0);
    Dp = Dm;
  }
  k_keep(keep[7], Dp, mine);
}
template <int LEFT>
__device__ __forceinline__ void v_tile(u32x4 (&X)[16], const u32x4 PW, u32 mine, f32x4 (&acc)[8], u32 voff, u32x4 srd, u32 soff, u32x2 t0, int n0) {
  u32x4 Bv[8];
  const u32x4 A = {PW.x & mine, PW.y & mine, PW.z & mine, PW.w & mine};
#pragma unroll
  for (int u = 0; u < 4; ++u) {
    WAIT4(X[4 * u], X[4 * u + 1], X[4 * u + 2], X[4 * u + 3], LEFT < 0 ? 28 : LEFT + 12 - 4 * u);
#pragma unroll
    for (int hf = 0; hf < 2; ++hf)
#pragma unroll
      for (int i = 0; i < 4; ++i) Bv[hf * 4 + i][u] = pk(X[4 * u + hf][i], X[4 * u + 2 + hf][i]);
    if (LEFT < 0) {
      PACE(DUE(t0, n0 + 4 * u));
#pragma unroll
      for (int n = 4 * u; n < 4 * u + 4; ++n) LDS_(X[n], voff, srd, soff, n * 256);
    }
    __builtin_amdgcn_sched_barrier(0);
  }
#pragma unroll
  for (int k = 0; k < 8; ++k) acc[k] = mfma(A, Bv[k], acc[k]);
  __builtin_amdgcn_sched_barrier(0);
}

__device__ __forceinline__ float row_max16(float x) {
  x = fmaxf(x, dpp<XOR1>(x)); x = fmaxf(x, dpp<XOR2>(x)); x = fmaxf(x, dpp<0x141>(x)); return fmaxf(x, dpp<ROR8>(x));
}
__device__ __forceinline__ float row_sum16(float x) {
  x += dpp<XOR1>(x); x += dpp<XOR2>(x); x += dpp<0x141>(x); return x + dpp<ROR8>(x);
}
constexpr u32 OOB = 0x80000000u;
constexpr u64 SPIN_CAP = 20000000ull;

__global__ void __launch_bounds__((WAVES + 1) * 64, 2)
kivi_decode(const uint16_t* __restrict__ q, const int* __restrict__ qk, const int* __restrict__ qv,
            const uint16_t* __restrict__ ks, const uint16_t* __restrict__ kz,
            const uint16_t* __restrict__ vs, const uint16_t* __restrict__ vz,
            unsigned char* ws, u32* __restrict__ out) {
  TS(0);
  u32x2 tw0 = {PACE_T ? (u32)__builtin_amdgcn_s_memrealtime() : 0u, PACE_T};
  __shared__ __attribute__((aligned(16))) float lds[WAVES * 4 * DH + WAVES * 16 + WAVES * 8 + 4];
  float* const s_acc = lds;
  float* const s_rec = lds + WAVES * 4 * DH;
  float* const s_fac = s_rec + WAVES * 16;
  constexpr u32 FAC_B = (WAVES * 4 * DH + WAVES * 16) * 4, FLAG_B = FAC_B + WAVES * 8 * 4;
  asm volatile("" :: "s"(q), "s"(qk), "s"(qv), "s"(ks), "s"(kz), "s"(vs), "s"(vz), "s"(ws), "s"(out));
  const int b = blockIdx.x, h = b >> 5, s = b & 31, grp = s >> 4;
  const int tid = threadIdx.x, lane = tid & 63, w = __builtin_amdgcn_readfirstlane(tid >> 6);

  if (w == WAVES) {
    volatile __attribute__((address_space(3))) u32* flag = (volatile __attribute__((address_space(3))) u32*)(s_fac + WAVES * 8);
    if (lane < 2) flag[lane] = 0u;
    __builtin_amdgcn_s_setprio(HPRIO);
    __syncthreads();
    const __amdgpu_buffer_rsrc_t rw = __builtin_amdgcn_make_buffer_rsrc((void*)ws, 0, WS_END, 0x00020000);
    u64* epp = (u64*)(ws + WS_EP + h * 256);
    const u64 ep = __hip_atomic_load(epp, __ATOMIC_RELAXED, __HIP_MEMORY_SCOPE_AGENT) ;
    const u32 tagA = __builtin_amdgcn_readfirstlane((u32)ep) + 1u, tagB = __builtin_amdgcn_readfirstlane((u32)(ep >> 32)) + 1u;
    if (lane == 0) { flag[2] = tagA; flag[3] = tagB; }
    while (flag[1] < WAVES) __builtin_amdgcn_s_sleep(8);
    asm volatile("" ::: "memory");
    const int hd = lane >> 4, p = lane & 15;
    {
      float M = -INFINITY, L = 0.f;
#pragma unroll
      for (int k = 0; k < WAVES; ++k) M = fmaxf(M, s_rec[(k * 4 + hd) * 4]);
#pragma unroll
      for (int k = 0; k < WAVES; ++k) L = fmaf(__builtin_amdgcn_exp2f(s_rec[(k * 4 + hd) * 4] - M), s_rec[(k * 4 + hd) * 4 + 1], L);
      const u32x4 gm = {__float_as_uint(M), tagA, __float_as_uint(L), tagB};
      __builtin_amdgcn_raw_buffer_store_b128(gm, rw, p == 0 ? (u32)(WS_ML + (h * NSPLIT + s) * 128 + hd * 16) : OOB, 0, 16);
    }
    u32x4 g0, g1;
    {
      const u32 mo = WS_ML + (h * NSPLIT + p) * 128 + hd * 16;
      if (MWAIT) { while ((int)((u32)__builtin_amdgcn_s_memrealtime() - tw0.x) < MWAIT) __builtin_amdgcn_s_sleep(8); }
      const u64 tstart = __builtin_amdgcn_s_memrealtime();
      bool ok0 = false, ok1 = false;
      for (;;) {
        const u32x4 x0 = __builtin_amdgcn_raw_buffer_load_b128(rw, ok0 ? OOB : mo, 0, 16), x1 = __builtin_amdgcn_raw_buffer_load_b128(rw, ok1 ? OOB : mo + 16 * 128, 0, 16);
        if (!ok0) { g0 = x0; ok0 = (x0.y == tagA) & (x0.w == tagB); }
        if (!ok1) { g1 = x1; ok1 = (x1.y == tagA) & (x1.w == tagB); }
        if (__all(ok0 & ok1) || __builtin_amdgcn_s_memrealtime() - tstart > SPIN_CAP) break;
        __builtin_amdgcn_s_sleep(MSLEEP);
        asm volatile("" ::: "memory");
      }
    }
    {
      const float m0 = __uint_as_float(g0.x), m1 = __uint_as_float(g1.x);
      const float M = row_max16(fmaxf(m0, m1));
      const float L = row_sum16(fmaf(__builtin_amdgcn_exp2f(m0 - M), __uint_as_float(g0.z), __builtin_amdgcn_exp2f(m1 - M) * __uint_as_float(g1.z)));
      if (p < WAVES) {
        const f32x4 rec = *(const f32x4*)(s_rec + (p * 4 + hd) * 4);
        const float f = __builtin_amdgcn_exp2f(rec[0] - M) / L;
        *(float2*)(s_fac + (p * 4 + hd) * 2) = make_float2(__builtin_amdgcn_ldexpf(f, __float_as_int(rec[3])), f * rec[2]);
      }
      asm volatile("s_waitcnt lgkmcnt(0)" ::: "memory");
      if (lane == 0) flag[0] = 1u;
      TS(1);
    }
    const u32 doff = WS_DAT + ((h * NSPLIT + s) * NSPLIT + (lane >> 3)) * 128 + (lane & 7) * 16;
    const u32x4 rws = make_srd(ws, WS_END);
    u32 oo = (((h * 4 + (s >> 3)) * DH + (s & 7) * 16) / 2 + (lane & 7)) * 4;
    asm volatile("" : "+v"(oo));
    float p0, p1;
    u32x4 ring[12];
    {
      u64 m0, m1, m2, m3, mt;
      u32 t0_;
      asm volatile(
          "s_not_b32 %7, %22\n\tv_mov_b32 v145, %7\n\tv_mov_b32 v149, %7\n\tv_mov_b32 v153, %7\n\tv_mov_b32 v157, %7\n\tv_mov_b32 v161, %7\n\tv_mov_b32 v165, %7\n\tv_mov_b32 v169, %7\n\tv_mov_b32 v173, %7\n\tv_mov_b32 v177, %7\n\tv_mov_b32 v181, %7\n\tv_mov_b32 v185, %7\n\tv_mov_b32 v189, %7\n\t"
          "s_mov_b64 %2, -1\n\ts_mov_b64 %3, -1\n\ts_mov_b64 %4, -1\n\ts_mov_b64 %5, -1\n\t"
          "s_memrealtime vcc\n\ts_waitcnt lgkmcnt(0)\n\ts_mov_b32 %7, vcc_lo\n\ts_barrier\n"
          "1:\n\t"
          "s_or_b64 exec, %2, 1\n\tbuffer_load_dwordx4 v[144:147], %20, %21, 0 offen sc1\n\t"
          "s_or_b64 exec, %3, 1\n\tbuffer_load_dwordx4 v[148:151], %20, %21, 0 offen offset:1024 sc1\n\t"
          "s_or_b64 exec, %4, 1\n\tbuffer_load_dwordx4 v[152:155], %20, %21, 0 offen offset:2048 sc1\n\t"
          "s_or_b64 exec, %5, 1\n\tbuffer_load_dwordx4 v[156:159], %20, %21, 0 offen offset:3072 sc1\n\t"
          "s_mov_b64 exec, -1\n\ts_sleep %c25\n\ts_waitcnt vmcnt(8)\n\t"
          "v_cmp_eq_u32 vcc, %22, v161\n\tv_cmp_eq_u32 %6, %23, v163\n\ts_and_b64 exec, vcc, %6\n\tv_mov_b32 v192, v160\n\tv_mov_b32 v193, v162\n\ts_andn2_b64 %2, %2, exec\n\ts_mov_b64 exec, -1\n\t"
          "v_cmp_eq_u32 vcc, %22, v165\n\tv_cmp_eq_u32 %6, %23, v167\n\ts_and_b64 exec, vcc, %6\n\tv_mov_b32 v194, v164\n\tv_mov_b32 v195, v166\n\ts_andn2_b64 %3, %3, exec\n\ts_mov_b64 exec, -1\n\t"
          "v_cmp_eq_u32 vcc, %22, v169\n\tv_cmp_eq_u32 %6, %23, v171\n\ts_and_b64 exec, vcc, %6\n\tv_mov_b32 v196, v168\n\tv_mov_b32 v197, v170\n\ts_andn2_b64 %4, %4, exec\n\ts_mov_b64 exec, -1\n\t"
          "v_cmp_eq_u32 vcc, %22, v173\n\tv_cmp_eq_u32 %6, %23, v175\n\ts_and_b64 exec, vcc, %6\n\tv_mov_b32 v198, v172\n\tv_mov_b32 v199, v174\n\ts_andn2_b64 %5, %5, exec\n\ts_mov_b64 exec, -1\n\t"
          "s_or_b64 vcc, %2, %3\n\ts_or_b64 vcc, vcc, %4\n\ts_or_b64 vcc, vcc, %5\n\ts_cbranch_scc0 2f\n\t"
          "s_or_b64 exec, %2, 1\n\tbuffer_load_dwordx4 v[160:163], %20, %21, 0 offen sc1\n\t"
          "s_or_b64 exec, %3, 1\n\tbuffer_load_dwordx4 v[164:167], %20, %21, 0 offen offset:1024 sc1\n\t"
          "s_or_b64 exec, %4, 1\n\tbuffer_load_dwordx4 v[168:171], %20, %21, 0 offen offset:2048 sc1\n\t"
          "s_or_b64 exec, %5, 1\n\tbuffer_load_dwordx4 v[172:175], %20, %21, 0 offen offset:3072 sc1\n\t"
          "s_mov_b64 exec, -1\n\ts_sleep %c25\n\ts_waitcnt vmcnt(8)\n\t"
          "v_cmp_eq_u32 vcc, %22, v177\n\tv_cmp_eq_u32 %6, %23, v179\n\ts_and_b64 exec, vcc, %6\n\tv_mov_b32 v192, v176\n\tv_mov_b32 v193, v178\n\ts_andn2_b64 %2, %2, exec\n\ts_mov_b64 exec, -1\n\t"
          "v_cmp_eq_u32 vcc, %22, v181\n\tv_cmp_eq_u32 %6, %23, v183\n\ts_and_b64 exec, vcc, %6\n\tv_mov_b32 v194, v180\n\tv_mov_b32 v195, v182\n\ts_andn2_b64 %3, %3, exec\n\ts_mov_b64 exec, -1\n\t"
          "v_cmp_eq_u32 vcc, %22, v185\n\tv_cmp_eq_u32 %6, %23, v187\n\ts_and_b64 exec, vcc, %6\n\tv_mov_b32 v196, v184\n\tv_mov_b32 v197, v186\n\ts_andn2_b64 %4, %4, exec\n\ts_mov_b64 exec, -1\n\t"
          "v_cmp_eq_u32 vcc, %22, v189\n\tv_cmp_eq_u32 %6, %23, v191\n\ts_and_b64 exec, vcc, %6\n\tv_mov_b32 v198, v188\n\tv_mov_b32 v199, v190\n\ts_andn2_b64 %5, %5, exec\n\ts_mov_b64 exec, -1\n\t"
          "s_or_b64 vcc, %2, %3\n\ts_or_b64 vcc, vcc, %4\n\ts_or_b64 vcc, vcc, %5\n\ts_cbranch_scc0 2f\n\t"
          "s_or_b64 exec, %2, 1\n\tbuffer_load_dwordx4 v[176:179], %20, %21, 0 offen sc1\n\t"
          "s_or_b64 exec, %3, 1\n\tbuffer_load_dwordx4 v[180:183], %20, %21, 0 offen offset:1024 sc1\n\t"
          "s_or_b64 exec, %4, 1\n\tbuffer_load_dwordx4 v[184:187], %20, %21, 0 offen offset:2048 sc1\n\t"
          "s_or_b64 exec, %5, 1\n\tbuffer_load_dwordx4 v[188:191], %20, %21, 0 offen offset:3072 sc1\n\t"
          "s_mov_b64 exec, -1\n\ts_sleep %c25\n\ts_waitcnt vmcnt(8)\n\t"
          "v_cmp_eq_u32 vcc, %22, v145\n\tv_cmp_eq_u32 %6, %23, v147\n\ts_and_b64 exec, vcc, %6\n\tv_mov_b32 v192, v144\n\tv_mov_b32 v193, v146\n\ts_andn2_b64 %2, %2, exec\n\ts_mov_b64 exec, -1\n\t"
          "v_cmp_eq_u32 vcc, %22, v149\n\tv_cmp_eq_u32 %6, %23, v151\n\ts_and_b64 exec, vcc, %6\n\tv_mov_b32 v194, v148\n\tv_mov_b32 v195, v150\n\ts_andn2_b64 %3, %3, exec\n\ts_mov_b64 exec, -1\n\t"
          "v_cmp_eq_u32 vcc, %22, v153\n\tv_cmp_eq_u32 %6, %23, v155\n\ts_and_b64 exec, vcc, %6\n\tv_mov_b32 v196, v152\n\tv_mov_b32 v197, v154\n\ts_andn2_b64 %4, %4, exec\n\ts_mov_b64 exec, -1\n\t"
          "v_cmp_eq_u32 vcc, %22, v157\n\tv_cmp_eq_u32 %6, %23, v159\n\ts_and_b64 exec, vcc, %6\n\tv_mov_b32 v198, v156\n\tv_mov_b32 v199, v158\n\ts_andn2_b64 %5, %5, exec\n\ts_mov_b64 exec, -1\n\t"
          "s_or_b64 vcc, %2, %3\n\ts_or_b64 vcc, vcc, %4\n\ts_or_b64 vcc, vcc, %5\n\ts_cbranch_scc0 2f\n\t"
          "s_memrealtime vcc\n\ts_waitcnt lgkmcnt(0)\n\ts_sub_u32 vcc_lo, vcc_lo, %7\n\ts_cmp_lt_u32 vcc_lo, %24\n\t"
          "s_cbranch_scc1 1b\n"
          "2:\n\t"
          "v_add_f32 %0, v192, v194\n\tv_add_f32 %1, v193, v195\n\tv_add_f32 v196, v196, v198\n\tv_add_f32 v197, v197, v199\n\t"
          "v_add_f32 %0, %0, v196\n\tv_add_f32 %1, %1, v197"
          : "=&v"(p0), "=&v"(p1), "=&s"(m0), "=&s"(m1), "=&s"(m2), "=&s"(m3), "=&s"(mt), "=&s"(t0_),
            "=&{v[144:147]}"(ring[0]), "=&{v[148:151]}"(ring[1]), "=&{v[152:155]}"(ring[2]), "=&{v[156:159]}"(ring[3]), "=&{v[160:163]}"(ring[4]), "=&{v[164:167]}"(ring[5]), "=&{v[168:171]}"(ring[6]), "=&{v[172:175]}"(ring[7]), "=&{v[176:179]}"(ring[8]), "=&{v[180:183]}"(ring[9]), "=&{v[184:187]}"(ring[10]), "=&{v[188:191]}"(ring[11])
          : "v"(doff), "s"(rws), "s"(tagA), "s"(tagB), "s"((u32)SPIN_CAP), "i"(POLL_SLEEP)
          : "vcc", "scc", "memory", "v192", "v193", "v194", "v195", "v196", "v197", "v198", "v199");
    }
    TS(5);
    const float O0 = red8_sum(p0), O1 = red8_sum(p1);
    if (lane < 8) { const bf16x2 o = {(__bf16)O0, (__bf16)O1}; *(u32*)((char*)out + oo) = __builtin_bit_cast(u32, o); }
    if (s == 0 && lane == 0) __hip_atomic_store(epp, ((u64)tagB << 32) | tagA, __ATOMIC_RELAXED, __HIP_MEMORY_SCOPE_AGENT);
    asm volatile("s_waitcnt vmcnt(0)" :: "{v[144:147]}"(ring[0]), "{v[148:151]}"(ring[1]), "{v[152:155]}"(ring[2]), "{v[156:159]}"(ring[3]), "{v[160:163]}"(ring[4]), "{v[164:167]}"(ring[5]), "{v[168:171]}"(ring[6]), "{v[172:175]}"(ring[7]), "{v[176:179]}"(ring[8]), "{v[180:183]}"(ring[9]), "{v[184:187]}"(ring[10]), "{v[188:191]}"(ring[11]) : "memory");
  } else {
  if (PACE_T && PACE_TO != PACE_T) { u32 xcc; asm volatile("s_getreg_b32 %0, hwreg(HW_REG_XCC_ID)" : "=s"(xcc)); tw0.y = (xcc & 1) ? PACE_TO : PACE_T; }
  const int g = lane >> 4, r = lane & 15, hh = r >> 2, pi = r & 3;
  const int tok0 = h * SKV + s * TOK_WG;
  const u32x4 rk = make_srd(qk, HKV * SKV * DH * 4), rv = make_srd(qv, HKV * SKV * DH * 4);
  const u32 koff = hh * 4096 + g * 64 + pi * 16;
  const u32 voff = g * 4096 + r * 16;
  const u32 wbase = (u32)(tok0 + w * 32) * 512u;
  u32x4 XA[16], XB[16];
  u32x2 q0, q1, s0, s1, z0, z1;
  u32x4 vsA, vsB, vzA, vzB;
  auto small = [&]() {
    const u32 d0 = (16 * g + 4 * pi) * 2;
    const uint16_t* qb = q + h * 4 * DH; const uint16_t* ksb = ks + (h * NGK + grp) * DH; const uint16_t* kzb = kz + (h * NGK + grp) * DH;
    const u32 qo = hh * (DH * 2) + d0;
    GLD2(q0, qo, qb, 0); GLD2(q1, qo, qb, 128); GLD2(s0, d0, ksb, 0); GLD2(s1, d0, ksb, 128); GLD2(z0, d0, kzb, 0); GLD2(z1, d0, kzb, 128);
    const u32 to = (u32)(pi * 2 * WAVES * 32 + 8 * g) * 2u;
    const uint16_t* vsb = vs + tok0 + w * 32; const uint16_t* vzb = vz + tok0 + w * 32;
    GLD4(vsA, to, vsb, 0); GLD4(vsB, to, vsb, WAVES * 32 * 2); GLD4(vzA, to, vzb, 0); GLD4(vzB, to, vzb, WAVES * 32 * 2);
  };
  if (SMALL_POS == 0) small();
#pragma unroll
  for (int n = 0; n < 16; ++n) { if (PACE_PRO && n % 4 == 0 && n > PACE_A) PACE(DUE(tw0, n)); LDN_(XA[n], koff, rk, wbase, n * 256); }
  if (SMALL_POS == 1) small();
  __builtin_amdgcn_s_barrier();
#pragma unroll
  for (int n = 0; n < 16; ++n) { if (PACE_PRO && n % 4 == 0 && n + 16 > PACE_A) PACE(DUE(tw0, n + 16)); LDN_(XB[n], koff, rk, wbase + TILE_STEP, n * 256); }
  asm volatile("s_waitcnt vmcnt(%10)" : "+v"(q0), "+v"(q1), "+v"(s0), "+v"(s1), "+v"(z0), "+v"(z1), "+v"(vsA), "+v"(vsB), "+v"(vzA), "+v"(vzB)
               : "i"(SMALL_POS == 0 ? 32 : 16) : "memory");
  float qq[8], zz[8];
  qq[0] = bf_lo(q0.x) * bf_lo(s0.x); qq[1] = bf_hi(q0.x) * bf_hi(s0.x); qq[2] = bf_lo(q0.y) * bf_lo(s0.y); qq[3] = bf_hi(q0.y) * bf_hi(s0.y);
  qq[4] = bf_lo(q1.x) * bf_lo(s1.x); qq[5] = bf_hi(q1.x) * bf_hi(s1.x); qq[6] = bf_lo(q1.y) * bf_lo(s1.y); qq[7] = bf_hi(q1.y) * bf_hi(s1.y);
  zz[0] = bf_lo(z0.x); zz[1] = bf_hi(z0.x); zz[2] = bf_lo(z0.y); zz[3] = bf_hi(z0.y);
  zz[4] = bf_lo(z1.x); zz[5] = bf_hi(z1.x); zz[6] = bf_lo(z1.y); zz[7] = bf_hi(z1.y);
  float qm = 0.f;
#pragma unroll
  for (int k = 0; k < 8; ++k) qm = fmaxf(qm, fabsf(qq[k]));
  qm = head_max(qm);
  const int eq = __builtin_amdgcn_frexp_expf(qm);
  u32x4 QB;
#pragma unroll
  for (int k = 0; k < 4; ++k) QB[k] = pkh(__builtin_amdgcn_ldexpf(qq[2 * k], 15 - eq), __builtin_amdgcn_ldexpf(qq[2 * k + 1], 15 - eq));
  float cq = 0.f;
#pragma unroll
  for (int k = 0; k < 4; ++k) { cq = fmaf(h_lo(QB[k]), zz[2 * k], cq); cq = fmaf(h_hi(QB[k]), zz[2 * k + 1], cq); }
  cq = head_sum(cq);
  constexpr float SC = 0.088388347648318440f * 1.4426950408889634f;
  const float c1 = __builtin_amdgcn_ldexpf(SC, eq + 9);
  const float cq2 = __builtin_amdgcn_ldexpf(cq, eq - 15) * SC;

  float kA[8], kB[8];
#pragma unroll
  for (int j = 0; j < 8; ++j) { kA[j] = 0.f; kB[j] = 0.f; }
#pragma unroll 1
  for (int i = 0; i < 4; ++i) {
    const bool lastk = i == 3;
    const u32x4 srd = lastk ? rv : rk;
    const u32 vo = lastk ? voff : koff;
    const u32 so = wbase + (lastk ? 0 : (2 * i + 2) * TILE_STEP);
    const u32 mine = pi == i ? ~0u : 0u;
    k_tile(XA, QB, kA, mine, vo, srd, so, tw0, 32 * i + 32);
    k_tile(XB, QB, kB, mine, vo, srd, so + TILE_STEP, tw0, 32 * i + 48);
  }
  TS(2);
  float mr = fmaxf(kA[0], kB[0]);
#pragma unroll
  for (int j = 1; j < 8; ++j) mr = fmaxf(mr, fmaxf(kA[j], kB[j]));
  mr = head_max(mr);
  float l = 0.f, wm = 0.f;
#pragma unroll
  for (int j = 0; j < 8; ++j) {
    const float pa = __builtin_amdgcn_exp2f((kA[j] - mr) * c1), pb = __builtin_amdgcn_exp2f((kB[j] - mr) * c1);
    l += pa + pb;
    kA[j] = pa * ((j & 1) ? bf_hi(vsA[j >> 1]) : bf_lo(vsA[j >> 1]));
    kB[j] = pb * ((j & 1) ? bf_hi(vsB[j >> 1]) : bf_lo(vsB[j >> 1]));
    wm = fmaxf(wm, fmaxf(fabsf(kA[j]), fabsf(kB[j])));
  }
  wm = head_max(wm);
  const int ew = __builtin_amdgcn_frexp_expf(wm);
  u32x4 PA, PB;
  float Z = 0.f;
#pragma unroll
  for (int k = 0; k < 4; ++k) {
    PA[k] = pkh(__builtin_amdgcn_ldexpf(kA[2 * k], 15 - ew), __builtin_amdgcn_ldexpf(kA[2 * k + 1], 15 - ew));
    PB[k] = pkh(__builtin_amdgcn_ldexpf(kB[2 * k], 15 - ew), __builtin_amdgcn_ldexpf(kB[2 * k + 1], 15 - ew));
    Z = fmaf(h_lo(PA[k]), bf_lo(vzA[k]), Z); Z = fmaf(h_hi(PA[k]), bf_hi(vzA[k]), Z);
    Z = fmaf(h_lo(PB[k]), bf_lo(vzB[k]), Z); Z = fmaf(h_hi(PB[k]), bf_hi(vzB[k]), Z);
  }
  l = head_sum(l); Z = head_sum(Z);
  if (lane < 16 && pi == 0) {
    f32x4 rec = {fmaf(mr, c1, -cq2), l, __builtin_amdgcn_ldexpf(Z, ew - 15), __int_as_float(ew + 9)};
    *(f32x4*)(s_rec + (w * 4 + hh) * 4) = rec;
  }
  {
    const u32 ca = lane == 0 ? 1u : 0u, cb = FLAG_B + 4;
    u64 sv;
    asm volatile("s_mov_b64 %0, exec\n\ts_mov_b64 exec, 1\n\tds_add_u32 %1, %2\n\ts_mov_b64 exec, %0" : "=&s"(sv) : "v"(cb), "v"(ca) : "memory");
  }
  f32x4 acc[8];
#pragma unroll
  for (int k = 0; k < 8; ++k) acc[k] = f32x4{0.f, 0.f, 0.f, 0.f};
#pragma unroll 1
  for (int i = 0; i < 3; ++i) {
    const u32 so = wbase + (2 * i + 2) * TILE_STEP;
    const u32 mine = pi == i ? ~0u : 0u;
    v_tile<-1>(XA, PA, mine, acc, voff, rv, so, tw0, 32 * i + 160);
    v_tile<-1>(XB, PB, mine, acc, voff, rv, so + TILE_STEP, tw0, 32 * i + 176);
  }
  if (LATEF) v_tile<16>(XA, PA, pi == 3 ? ~0u : 0u, acc, voff, rv, 0, tw0, 0);
  u32x2 fac, tg;
  {
    u32 fl, sf;
    const u32 fa_ = FAC_B + (w * 4 + g) * 8, fl_ = FLAG_B;
    asm volatile("1:\n\tds_read_b32 %1, %5\n\tds_read_b64 %0, %4\n\tds_read_b64 %3, %5 offset:8\n\ts_waitcnt lgkmcnt(0)\n\tv_readfirstlane_b32 %2, %1\n\ts_cmp_lg_u32 %2, 0\n\ts_cbranch_scc1 2f\n\ts_sleep 2\n\ts_branch 1b\n2:"
                 : "=&v"(fac), "=&v"(fl), "=&s"(sf), "=&v"(tg) : "v"(fa_), "v"(fl_) : "scc", "memory");
  }
  const float fa = __uint_as_float(fac.x), fz = __uint_as_float(fac.y);
  float pre[8];
#pragma unroll
  for (int k = 0; k < 8; ++k) pre[k] = fmaf((acc[k][0] + acc[k][1]) + acc[k][2], fa, -fz);
  u32 sto = WS_DAT + ((h * NSPLIT + w * 8 + (lane >> 3)) * NSPLIT + s) * 128 + (lane & 7) * 16;
  u32 tgA = tg.x, tgB = tg.y, la0 = ((w * 4 + g) * DH + 4 * r) * 4, la1 = (w * DH + 2 * lane) * 4;
  asm volatile("" : "+v"(pre[0]), "+v"(pre[1]), "+v"(pre[2]), "+v"(pre[3]), "+v"(pre[4]), "+v"(pre[5]), "+v"(pre[6]), "+v"(pre[7]),
               "+v"(sto), "+v"(tgA), "+v"(tgB), "+v"(la0), "+v"(la1));
  __builtin_amdgcn_sched_barrier(0);
  if (!LATEF) v_tile<16>(XA, PA, pi == 3 ? ~0u : 0u, acc, voff, rv, 0, tw0, 0);
  v_tile<0>(XB, PB, pi == 3 ? ~0u : 0u, acc, voff, rv, 0, tw0, 0);
  TS(3);
  {
    f32x4 o0, o1;
#pragma unroll
    for (int i = 0; i < 4; ++i) { o0[i] = fmaf(acc[i][3], fa, pre[i]); o1[i] = fmaf(acc[4 + i][3], fa, pre[4 + i]); }
    float* dst = (float*)((char*)s_acc + la0);
    *(f32x4*)dst = o0; *(f32x4*)(dst + 64) = o1;
  }
  __syncthreads();
  {
    float2 a[WAVES];
#pragma unroll
    for (int k = 0; k < WAVES; ++k) a[k] = *(const float2*)((const char*)s_acc + la1 + k * 4 * DH * 4);
    const float O0 = (a[0].x + a[1].x) + (a[2].x + a[3].x), O1 = (a[0].y + a[1].y) + (a[2].y + a[3].y);
    const u32x4 gd = {__float_as_uint(O0), tgA, __float_as_uint(O1), tgB};
    const __amdgpu_buffer_rsrc_t rw = __builtin_amdgcn_make_buffer_rsrc((void*)ws, 0, WS_END, 0x00020000);
    __builtin_amdgcn_raw_buffer_store_b128(gd, rw, sto, 0, 16);
  }
  TS(4);
  }
}

extern "C" void kernel_launch(void* const* d_in, const int* in_sizes, int n_in, void* d_out, int out_size,
                              void* d_ws, size_t ws_size, hipStream_t stream) {
  kivi_decode<<<HKV * NSPLIT, (WAVES + 1) * 64, 0, stream>>>((const uint16_t*)d_in[0], (const int*)d_in[1], (const int*)d_in[2],
      (const uint16_t*)d_in[3], (const uint16_t*)d_in[4], (const uint16_t*)d_in[5], (const uint16_t*)d_in[6], (unsigned char*)d_ws, (u32*)d_out);
}
```

```cpp
#include <hip/hip_runtime.h>
#include <hip/hip_bf16.h>
#include <stdint.h>

typedef unsigned u32;
typedef unsigned long long u64;
typedef u32 u32x4 __attribute__((ext_vector_type(4)));
typedef u32 u32x2 __attribute__((ext_vector_type(2)));
typedef float f32x4 __attribute__((ext_vector_type(4)));
typedef _Float16 f16x8 __attribute__((ext_vector_type(8)));
typedef _Float16 f16x2 __attribute__((ext_vector_type(2)));
typedef __bf16 bf16x2 __attribute__((ext_vector_type(2)));
typedef volatile __attribute__((address_space(3))) u32 lds_word;

constexpr int HQ = 32, HKV = 8, SKV = 32768, DH = 128, NGK = 2;
constexpr int NSPLIT = 32;
constexpr int WAVES = 4;
constexpr int TOK_WG = SKV / NSPLIT;
constexpr int TILE_ROWS = 4 * WAVES * 32;
constexpr int TILE_STEP = TILE_ROWS * 512;

#define PACE_T 3785
#define PACE_TO 3750
constexpr int PACE_A = 12;
constexpr int STAT_LOOK = 2800;
constexpr int POLL_DELAY = 18;

constexpr int WS_EP = 0, WS_ML = 4096, WS_DAT = WS_ML + HKV * NSPLIT * 128, WS_END = WS_DAT + HKV * NSPLIT * NSPLIT * 128;
constexpr u32 L_ACC = 0;
constexpr u32 L_VS = L_ACC + WAVES * 4 * DH * 4;
constexpr u32 L_QB = L_VS + 2 * TOK_WG * 2;
constexpr u32 L_QC = L_QB + 64 * 16;
constexpr u32 L_REC = L_QC + 4 * 8;
constexpr u32 L_FAC = L_REC + WAVES * 4 * 16;
constexpr u32 L_FLAG = L_FAC + WAVES * 4 * 8;
constexpr u32 L_END = L_FLAG + 16;

constexpr u32 OOB = 0x80000000u;
constexpr u32 SPIN_CAP = 20000000u;
constexpr int SC1 = 16;

__device__ __forceinline__ float bf_lo(u32 x) { return __uint_as_float(x << 16); }
__device__ __forceinline__ float bf_hi(u32 x) { return __uint_as_float(x & 0xffff0000u); }
__device__ __forceinline__ u32 pk(u32 a, u32 b) { return a | (b << 16); }
template <int CTRL> __device__ __forceinline__ float dpp(float x) {
  return __int_as_float(__builtin_amdgcn_mov_dpp(__float_as_int(x), CTRL, 0xf, 0xf, true));
}
constexpr int XOR1 = 0xB1, XOR2 = 0x4E, HMIR = 0x141, ROR8 = 0x128;
__device__ __forceinline__ float rows_max(float x) {
  auto s = __builtin_amdgcn_permlane16_swap(__float_as_uint(x), __float_as_uint(x), false, false);
  x = fmaxf(__uint_as_float(s[0]), __uint_as_float(s[1]));
  auto t = __builtin_amdgcn_permlane32_swap(__float_as_uint(x), __float_as_uint(x), false, false);
  return fmaxf(__uint_as_float(t[0]), __uint_as_float(t[1]));
}
__device__ __forceinline__ float rows_sum(float x) {
  auto s = __builtin_amdgcn_permlane16_swap(__float_as_uint(x), __float_as_uint(x), false, false);
  x = __uint_as_float(s[0]) + __uint_as_float(s[1]);
  auto t = __builtin_amdgcn_permlane32_swap(__float_as_uint(x), __float_as_uint(x), false, false);
  return __uint_as_float(t[0]) + __uint_as_float(t[1]);
}
__device__ __forceinline__ float head_max(float x) { x = fmaxf(x, dpp<XOR1>(x)); x = fmaxf(x, dpp<XOR2>(x)); return rows_max(x); }
__device__ __forceinline__ float head_sum(float x) { x += dpp<XOR1>(x); x += dpp<XOR2>(x); return rows_sum(x); }
__device__ __forceinline__ float red8_sum(float x) { return rows_sum(x + dpp<ROR8>(x)); }
__device__ __forceinline__ float row_max16(float x) { x = fmaxf(x, dpp<XOR1>(x)); x = fmaxf(x, dpp<XOR2>(x)); x = fmaxf(x, dpp<HMIR>(x)); return fmaxf(x, dpp<ROR8>(x)); }
__device__ __forceinline__ float row_sum16(float x) { x += dpp<XOR1>(x); x += dpp<XOR2>(x); x += dpp<HMIR>(x); return x + dpp<ROR8>(x); }
__device__ __forceinline__ u32 pkh(float a, float b) { f16x2 h = {(_Float16)a, (_Float16)b}; return __builtin_bit_cast(u32, h); }
__device__ __forceinline__ float h_lo(u32 x) { return (float)__builtin_bit_cast(f16x2, x)[0]; }
__device__ __forceinline__ float h_hi(u32 x) { return (float)__builtin_bit_cast(f16x2, x)[1]; }
__device__ __forceinline__ f32x4 mfma(u32x4 a, u32x4 b, f32x4 c) {
  return __builtin_amdgcn_mfma_f32_16x16x32_f16(__builtin_bit_cast(f16x8, a), __builtin_bit_cast(f16x8, b), c, 0, 0, 0);
}
__device__ __forceinline__ u32x4 make_srd(const void* p, u32 bytes) {
  const u64 a = (u64)p;
  return u32x4{(u32)a, (u32)(a >> 32) & 0xffffu, bytes, 0x00020000u};
}
__device__ __forceinline__ u32 now() { return (u32)__builtin_amdgcn_s_memrealtime(); }

#define LD_TILE(C, reg, voff, srd, soff, n) \
  asm volatile("buffer_load_dwordx4 %0, %1, %2, %3 offen offset:%4 nt" : C(reg) : "v"((voff) ^ (((n) & 12) << 8)), "s"(srd), "s"(soff), "i"(((n) & 3) * 256) : "memory")
#define WAIT2(a, b, n) asm volatile("s_waitcnt vmcnt(%2)" : "+v"(a), "+v"(b) : "i"(n) : "memory")
#define WAIT4(a, b, c, d, n) asm volatile("s_waitcnt vmcnt(%4)" : "+v"(a), "+v"(b), "+v"(c), "+v"(d) : "i"(n) : "memory")
#define PACE(t0, n) \
  asm volatile("1:\n\ts_memrealtime vcc\n\ts_waitcnt lgkmcnt(0)\n\ts_sub_i32 vcc_lo, vcc_lo, %0\n\ts_cmp_ge_i32 vcc_lo, 0\n\ts_cbranch_scc1 2f\n\ts_sleep 1\n\ts_branch 1b\n2:" \
               :: "s"((t0).x + (u32)((((int)(n) - PACE_A) * (int)(t0).y) >> 8)) : "scc", "vcc", "memory")
#define WAIT_STAGE(stage) do { u32 v_, s_; const u32 a_ = L_FLAG; \
  asm volatile("1:\n\tds_read_b32 %0, %2\n\ts_waitcnt lgkmcnt(0)\n\tv_readfirstlane_b32 %1, %0\n\ts_cmp_ge_u32 %1, %3\n\ts_cbranch_scc1 2f\n\ts_sleep 1\n\ts_branch 1b\n2:" \
               : "=&v"(v_), "=&s"(s_) : "v"(a_), "i"(stage) : "scc", "memory"); } while (0)

__device__ __forceinline__ void k_keep(float& keep, const f32x4 Dm, u32 mine) {
  const float S = ((dpp<0x00>(Dm[0]) + dpp<0x55>(Dm[1])) + dpp<0xAA>(Dm[2])) + dpp<0xFF>(Dm[3]);
  keep = __uint_as_float((__float_as_uint(S) & mine) | (__float_as_uint(keep) & ~mine));
}
__device__ __forceinline__ void k_tile(u32x4 (&X)[16], const u32x4 QB, float (&keep)[8], u32 mine, u32 voff, u32x4 srd, u32 soff, u32x2 t0, int n0) {
  f32x4 Dp;
#pragma unroll
  for (int j = 0; j < 8; ++j) {
    WAIT2(X[2 * j], X[2 * j + 1], 30);
    const u32x4 x = X[2 * j], y = X[2 * j + 1];
    const u32x4 A = {pk(x.x, x.y), pk(x.z, x.w), pk(y.x, y.y), pk(y.z, y.w)};
    if ((j & 1) == 0) PACE(t0, n0 + 2 * j);
    LD_TILE("+v", X[2 * j], voff, srd, soff, 2 * j);
    LD_TILE("+v", X[2 * j + 1], voff, srd, soff, 2 * j + 1);
    const f32x4 Dm = mfma(A, QB, f32x4{0.f, 0.f, 0.f, 0.f});
    __builtin_amdgcn_sched_barrier(0);
    if (j > 0) k_keep(keep[j - 1], Dp, mine);
    __builtin_amdgcn_sched_barrier(0);
    Dp = Dm;
  }
  k_keep(keep[7], Dp, mine);
}
template <int LEFT>
__device__ __forceinline__ void v_tile(u32x4 (&X)[16], const u32x4 PW, u32 mine, f32x4 (&acc)[8], u32 voff, u32x4 srd, u32 soff, u32x2 t0, int n0) {
  u32x4 Bv[8];
  const u32x4 A = {PW.x & mine, PW.y & mine, PW.z & mine, PW.w & mine};
#pragma unroll
  for (int u = 0; u < 4; ++u) {
    WAIT4(X[4 * u], X[4 * u + 1], X[4 * u + 2], X[4 * u + 3], LEFT < 0 ? 28 : LEFT + 12 - 4 * u);
#pragma unroll
    for (int hf = 0; hf < 2; ++hf)
#pragma unroll
      for (int i = 0; i < 4; ++i) Bv[hf * 4 + i][u] = pk(X[4 * u + hf][i], X[4 * u + 2 + hf][i]);
    if (LEFT < 0) {
      PACE(t0, n0 + 4 * u);
#pragma unroll
      for (int n = 4 * u; n < 4 * u + 4; ++n) LD_TILE("+v", X[n], voff, srd, soff, n);
    }
    __builtin_amdgcn_sched_barrier(0);
  }
#pragma unroll
  for (int k = 0; k < 8; ++k) acc[k] = mfma(A, Bv[k], acc[k]);
  __builtin_amdgcn_sched_barrier(0);
}

__global__ void __launch_bounds__((WAVES + 1) * 64, 2)
kivi_decode(const uint16_t* __restrict__ q, const int* __restrict__ qk, const int* __restrict__ qv,
            const uint16_t* __restrict__ ks, const uint16_t* __restrict__ kz,
            const uint16_t* __restrict__ vs, const uint16_t* __restrict__ vz,
            unsigned char* ws, u32* __restrict__ out) {
  __shared__ __attribute__((aligned(16))) unsigned char lds[L_END];
  u32x2 tw0 = {now(), PACE_T};
  asm volatile("" :: "s"(q), "s"(qk), "s"(qv), "s"(ks), "s"(kz), "s"(vs), "s"(vz), "s"(ws), "s"(out));
  const int b = blockIdx.x, h = b >> 5;
  const int s = ((b & 3) * 2 + ((b >> 4) & 1)) * 4 + ((b >> 2) & 1) * 2 + ((b >> 3) & 1);
  const int tid = threadIdx.x, lane = tid & 63, w = __builtin_amdgcn_readfirstlane(tid >> 6);
  const int g = lane >> 4, r = lane & 15, hh = r >> 2, pi = r & 3;
  const int tok0 = h * SKV + (s >> 2) * (4 * TOK_WG) + (s & 3) * (WAVES * 32);

  if (w == WAVES) {
    lds_word* flag = (lds_word*)(lds + L_FLAG);
    __builtin_amdgcn_s_setprio(3);
    if (lane < 2) flag[lane] = 0u;
    const int d0 = 16 * g + 4 * pi;
    const uint16_t* qp = q + (h * 4 + hh) * DH + d0;
    const uint16_t* ksp = ks + (h * NGK + (s >> 4)) * DH + d0;
    const uint16_t* kzp = kz + (h * NGK + (s >> 4)) * DH + d0;
    const u32x2 q0 = *(const u32x2*)qp, q1 = *(const u32x2*)(qp + 64), s0 = *(const u32x2*)ksp, s1 = *(const u32x2*)(ksp + 64);
    const u32x2 z0 = *(const u32x2*)kzp, z1 = *(const u32x2*)(kzp + 64);
    asm volatile("s_waitcnt lgkmcnt(0)\n\ts_barrier" ::: "memory");
    {
      const float qq[8] = {bf_lo(q0.x) * bf_lo(s0.x), bf_hi(q0.x) * bf_hi(s0.x), bf_lo(q0.y) * bf_lo(s0.y), bf_hi(q0.y) * bf_hi(s0.y),
                           bf_lo(q1.x) * bf_lo(s1.x), bf_hi(q1.x) * bf_hi(s1.x), bf_lo(q1.y) * bf_lo(s1.y), bf_hi(q1.y) * bf_hi(s1.y)};
      const float zz[8] = {bf_lo(z0.x), bf_hi(z0.x), bf_lo(z0.y), bf_hi(z0.y), bf_lo(z1.x), bf_hi(z1.x), bf_lo(z1.y), bf_hi(z1.y)};
      float qm = 0.f;
#pragma unroll
      for (int k = 0; k < 8; ++k) qm = fmaxf(qm, fabsf(qq[k]));
      const int eq = __builtin_amdgcn_frexp_expf(head_max(qm));
      u32x4 QB;
#pragma unroll
      for (int k = 0; k < 4; ++k) QB[k] = pkh(__builtin_amdgcn_ldexpf(qq[2 * k], 15 - eq), __builtin_amdgcn_ldexpf(qq[2 * k + 1], 15 - eq));
      *(u32x4*)(lds + L_QB + lane * 16) = QB;
      asm volatile("s_waitcnt lgkmcnt(0)" ::: "memory");
      if (lane == 0) flag[0] = 1u;
      float cq = 0.f;
#pragma unroll
      for (int k = 0; k < 4; ++k) { cq = fmaf(h_lo(QB[k]), zz[2 * k], cq); cq = fmaf(h_hi(QB[k]), zz[2 * k + 1], cq); }
      cq = head_sum(cq);
      constexpr float SC = 0.088388347648318440f * 1.4426950408889634f;
      if (lane < 16 && pi == 0) *(float2*)(lds + L_QC + hh * 8) = make_float2(__builtin_amdgcn_ldexpf(SC, eq + 9), __builtin_amdgcn_ldexpf(cq, eq - 15) * SC);
    }
    u64* epp = (u64*)(ws + WS_EP + h * 256);
    const u64 ep = __hip_atomic_load(epp, __ATOMIC_RELAXED, __HIP_MEMORY_SCOPE_AGENT);
    {
      u32x4 t[4];
#pragma unroll
      for (int k = 0; k < 4; ++k) t[k] = __builtin_nontemporal_load((const u32x4*)((k < 2 ? vs : vz) + tok0 + ((k & 1) * 4 + g) * TILE_ROWS + r * 8));
#pragma unroll
      for (int k = 0; k < 4; ++k) *(u32x4*)(lds + L_VS + k * 1024 + lane * 16) = t[k];
    }
    const u32 tagA = __builtin_amdgcn_readfirstlane((u32)ep) + 1u, tagB = __builtin_amdgcn_readfirstlane((u32)(ep >> 32)) + 1u;
    if (lane == 0) { flag[2] = tagA; flag[3] = tagB; }
    asm volatile("s_waitcnt lgkmcnt(0)" ::: "memory");
    if (lane == 0) flag[0] = 2u;
    while (flag[1] < WAVES) __builtin_amdgcn_s_sleep(8);
    asm volatile("" ::: "memory");
    const __amdgpu_buffer_rsrc_t rw = __builtin_amdgcn_make_buffer_rsrc((void*)ws, 0, WS_END, 0x00020000);
    const float* rec = (const float*)(lds + L_REC);
    {
      float M = -INFINITY, L = 0.f;
#pragma unroll
      for (int k = 0; k < WAVES; ++k) M = fmaxf(M, rec[(k * 4 + g) * 4]);
#pragma unroll
      for (int k = 0; k < WAVES; ++k) L = fmaf(__builtin_amdgcn_exp2f(rec[(k * 4 + g) * 4] - M), rec[(k * 4 + g) * 4 + 1], L);
      const u32x4 gm = {__float_as_uint(M), tagA, __float_as_uint(L), tagB};
      __builtin_amdgcn_raw_buffer_store_b128(gm, rw, r == 0 ? (u32)(WS_ML + (h * NSPLIT + s) * 128 + g * 16) : OOB, 0, SC1);
    }
    while ((int)(now() - tw0.x) < STAT_LOOK) __builtin_amdgcn_s_sleep(8);
    {
      const u32 mo = WS_ML + (h * NSPLIT + r) * 128 + g * 16, tstart = now();
      u32x4 g0, g1;
      bool ok0 = false, ok1 = false;
      for (;;) {
        const u32x4 x0 = __builtin_amdgcn_raw_buffer_load_b128(rw, ok0 ? OOB : mo, 0, SC1);
        const u32x4 x1 = __builtin_amdgcn_raw_buffer_load_b128(rw, ok1 ? OOB : mo + 16 * 128, 0, SC1);
        if (!ok0) { g0 = x0; ok0 = (x0.y == tagA) & (x0.w == tagB); }
        if (!ok1) { g1 = x1; ok1 = (x1.y == tagA) & (x1.w == tagB); }
        if (__all(ok0 & ok1) || now() - tstart > SPIN_CAP) break;
        __builtin_amdgcn_s_sleep(32);
        asm volatile("" ::: "memory");
      }
      const float m0 = __uint_as_float(g0.x), m1 = __uint_as_float(g1.x);
      const float M = row_max16(fmaxf(m0, m1));
      const float L = row_sum16(fmaf(__builtin_amdgcn_exp2f(m0 - M), __uint_as_float(g0.z), __builtin_amdgcn_exp2f(m1 - M) * __uint_as_float(g1.z)));
      if (r < WAVES) {
        const f32x4 rc = *(const f32x4*)(rec + (r * 4 + g) * 4);
        const float f = __builtin_amdgcn_exp2f(rc[0] - M) / L;
        *(float2*)(lds + L_FAC + (r * 4 + g) * 8) = make_float2(__builtin_amdgcn_ldexpf(f, __float_as_int(rc[3])), f * rc[2]);
      }
      asm volatile("s_waitcnt lgkmcnt(0)" ::: "memory");
      if (lane == 0) flag[0] = 3u;
    }
    const u32 doff = WS_DAT + ((h * NSPLIT + s) * NSPLIT + (lane >> 3)) * 128 + (lane & 7) * 16;
    const u32x4 rws = make_srd(ws, WS_END);
    const __amdgpu_buffer_rsrc_t ro = __builtin_amdgcn_make_buffer_rsrc((void*)out, 0, HQ * DH * 2, 0x00020000);
    u32 oo = lane < 8 ? (u32)((((h * 4 + (s >> 3)) * DH + (s & 7) * 16) / 2 + lane) * 4) : OOB;
    asm volatile("" : "+v"(oo));
    float p0, p1;
    u32x4 ring[12];
    {
      u64 m0, m1, m2, m3, mt;
      u32 t0_;
#define POLL_TAKE(m, x, y, z, w_, d0_, d1_) \
  "v_cmp_eq_u32 vcc, %22, v" #y "\n\tv_cmp_eq_u32 %6, %23, v" #w_ "\n\ts_and_b64 exec, vcc, %6\n\t" \
  "v_mov_b32 v" #d0_ ", v" #x "\n\tv_mov_b32 v" #d1_ ", v" #z "\n\ts_andn2_b64 %" #m ", %" #m ", exec\n\ts_mov_b64 exec, -1\n\t"
      asm volatile(
          "s_not_b32 %7, %22\n\t"
          "v_mov_b32 v65, %7\n\tv_mov_b32 v69, %7\n\tv_mov_b32 v73, %7\n\tv_mov_b32 v77, %7\n\t"
          "v_mov_b32 v81, %7\n\tv_mov_b32 v85, %7\n\tv_mov_b32 v89, %7\n\tv_mov_b32 v93, %7\n\t"
          "v_mov_b32 v97, %7\n\tv_mov_b32 v101, %7\n\tv_mov_b32 v105, %7\n\tv_mov_b32 v109, %7\n\t"
          "s_mov_b64 %2, -1\n\ts_mov_b64 %3, -1\n\ts_mov_b64 %4, -1\n\ts_mov_b64 %5, -1\n\t"
          "s_memrealtime vcc\n\ts_waitcnt lgkmcnt(0)\n\ts_mov_b32 %7, vcc_lo\n\t"
          "s_barrier\n\ts_sleep %c25\n"
          "1:\n\t"
          "s_or_b64 exec, %2, 1\n\tbuffer_load_dwordx4 v[64:67], %20, %21, 0 offen sc1\n\t"
          "s_or_b64 exec, %3, 1\n\tbuffer_load_dwordx4 v[68:71], %20, %21, 0 offen offset:1024 sc1\n\t"
          "s_or_b64 exec, %4, 1\n\tbuffer_load_dwordx4 v[72:75], %20, %21, 0 offen offset:2048 sc1\n\t"
          "s_or_b64 exec, %5, 1\n\tbuffer_load_dwordx4 v[76:79], %20, %21, 0 offen offset:3072 sc1\n\t"
          "s_mov_b64 exec, -1\n\ts_waitcnt vmcnt(8)\n\t"
          POLL_TAKE(2, 80, 81, 82, 83, 112, 113)
          POLL_TAKE(3, 84, 85, 86, 87, 114, 115)
          POLL_TAKE(4, 88, 89, 90, 91, 116, 117)
          POLL_TAKE(5, 92, 93, 94, 95, 118, 119)
          "s_or_b64 vcc, %2, %3\n\ts_or_b64 vcc, vcc, %4\n\ts_or_b64 vcc, vcc, %5\n\ts_cbranch_scc0 2f\n\t"
          "s_or_b64 exec, %2, 1\n\tbuffer_load_dwordx4 v[80:83], %20, %21, 0 offen sc1\n\t"
          "s_or_b64 exec, %3, 1\n\tbuffer_load_dwordx4 v[84:87], %20, %21, 0 offen offset:1024 sc1\n\t"
          "s_or_b64 exec, %4, 1\n\tbuffer_load_dwordx4 v[88:91], %20, %21, 0 offen offset:2048 sc1\n\t"
          "s_or_b64 exec, %5, 1\n\tbuffer_load_dwordx4 v[92:95], %20, %21, 0 offen offset:3072 sc1\n\t"
          "s_mov_b64 exec, -1\n\ts_waitcnt vmcnt(8)\n\t"
          POLL_TAKE(2, 96, 97, 98, 99, 112, 113)
          POLL_TAKE(3, 100, 101, 102, 103, 114, 115)
          POLL_TAKE(4, 104, 105, 106, 107, 116, 117)
          POLL_TAKE(5, 108, 109, 110, 111, 118, 119)
          "s_or_b64 vcc, %2, %3\n\ts_or_b64 vcc, vcc, %4\n\ts_or_b64 vcc, vcc, %5\n\ts_cbranch_scc0 2f\n\t"
          "s_or_b64 exec, %2, 1\n\tbuffer_load_dwordx4 v[96:99], %20, %21, 0 offen sc1\n\t"
          "s_or_b64 exec, %3, 1\n\tbuffer_load_dwordx4 v[100:103], %20, %21, 0 offen offset:1024 sc1\n\t"
          "s_or_b64 exec, %4, 1\n\tbuffer_load_dwordx4 v[104:107], %20, %21, 0 offen offset:2048 sc1\n\t"
          "s_or_b64 exec, %5, 1\n\tbuffer_load_dwordx4 v[108:111], %20, %21, 0 offen offset:3072 sc1\n\t"
          "s_mov_b64 exec, -1\n\ts_waitcnt vmcnt(8)\n\t"
          POLL_TAKE(2, 64, 65, 66, 67, 112, 113)
          POLL_TAKE(3, 68, 69, 70, 71, 114, 115)
          POLL_TAKE(4, 72, 73, 74, 75, 116, 117)
          POLL_TAKE(5, 76, 77, 78, 79, 118, 119)
          "s_or_b64 vcc, %2, %3\n\ts_or_b64 vcc, vcc, %4\n\ts_or_b64 vcc, vcc, %5\n\ts_cbranch_scc0 2f\n\t"
          "s_memrealtime vcc\n\ts_waitcnt lgkmcnt(0)\n\ts_sub_u32 vcc_lo, vcc_lo, %7\n\ts_cmp_lt_u32 vcc_lo, %24\n\t"
          "s_cbranch_scc1 1b\n"
          "2:\n\t"
          "v_add_f32 %0, v112, v114\n\tv_add_f32 %1, v113, v115\n\tv_add_f32 v116, v116, v118\n\tv_add_f32 v117, v117, v119\n\t"
          "v_add_f32 %0, %0, v116\n\tv_add_f32 %1, %1, v117"
          : "=&v"(p0), "=&v"(p1), "=&s"(m0), "=&s"(m1), "=&s"(m2), "=&s"(m3), "=&s"(mt), "=&s"(t0_),
            "=&{v[64:67]}"(ring[0]), "=&{v[68:71]}"(ring[1]), "=&{v[72:75]}"(ring[2]), "=&{v[76:79]}"(ring[3]), "=&{v[80:83]}"(ring[4]),
            "=&{v[84:87]}"(ring[5]), "=&{v[88:91]}"(ring[6]), "=&{v[92:95]}"(ring[7]), "=&{v[96:99]}"(ring[8]), "=&{v[100:103]}"(ring[9]),
            "=&{v[104:107]}"(ring[10]), "=&{v[108:111]}"(ring[11])
          : "v"(doff), "s"(rws), "s"(tagA), "s"(tagB), "s"(SPIN_CAP), "i"(POLL_DELAY)
          : "vcc", "scc", "memory", "v112", "v113", "v114", "v115", "v116", "v117", "v118", "v119");
    }
    const bf16x2 o = {(__bf16)red8_sum(p0), (__bf16)red8_sum(p1)};
    __builtin_amdgcn_raw_buffer_store_b32(__builtin_bit_cast(u32, o), ro, oo, 0, 0);
    if (s == 0 && lane == 0) __hip_atomic_store(epp, ((u64)tagB << 32) | tagA, __ATOMIC_RELAXED, __HIP_MEMORY_SCOPE_AGENT);
    asm volatile("s_waitcnt vmcnt(0)"
                 :: "{v[64:67]}"(ring[0]), "{v[68:71]}"(ring[1]), "{v[72:75]}"(ring[2]), "{v[76:79]}"(ring[3]), "{v[80:83]}"(ring[4]),
                 "{v[84:87]}"(ring[5]), "{v[88:91]}"(ring[6]), "{v[92:95]}"(ring[7]), "{v[96:99]}"(ring[8]), "{v[100:103]}"(ring[9]),
                 "{v[104:107]}"(ring[10]), "{v[108:111]}"(ring[11]) : "memory");
  } else {
    { u32 xcc; asm volatile("s_getreg_b32 %0, hwreg(HW_REG_XCC_ID)" : "=s"(xcc)); tw0.y = (xcc & 1) ? PACE_TO : PACE_T; }
    const u32x4 rk = make_srd(qk, HKV * SKV * DH * 4), rv = make_srd(qv, HKV * SKV * DH * 4);
    const u32 koff = (w << 10) + hh * 4096 + g * 64 + pi * 16;
    const u32 voff = (w << 10) + g * 4096 + r * 16;
    const u32 wbase = (u32)(tok0 + w * 32) * 512u;
    u32x4 XA[16], XB[16];
#pragma unroll
    for (int n = 0; n < 16; ++n) {
      if (n == 12) __builtin_amdgcn_s_barrier();
      LD_TILE("=v", XA[n], koff, rk, wbase, n);
    }
#pragma unroll
    for (int n = 0; n < 16; ++n) { if (n % 4 == 0) PACE(tw0, n + 16); LD_TILE("=v", XB[n], koff, rk, wbase + TILE_STEP, n); }
    WAIT_STAGE(1);
    const u32x4 QB = *(const u32x4*)(lds + L_QB + lane * 16);

    float kA[8], kB[8];
#pragma unroll
    for (int j = 0; j < 8; ++j) { kA[j] = 0.f; kB[j] = 0.f; }
#pragma unroll 1
    for (int i = 0; i < 4; ++i) {
      const bool lastk = i == 3;
      const u32x4 srd = lastk ? rv : rk;
      const u32 vo = lastk ? voff : koff;
      const u32 so = wbase + ((2 * i + 2) & 7) * TILE_STEP;
      const u32 mine = pi == i ? ~0u : 0u;
      k_tile(XA, QB, kA, mine, vo, srd, so, tw0, 32 * i + 32);
      k_tile(XB, QB, kB, mine, vo, srd, so + TILE_STEP, tw0, 32 * i + 48);
    }
    WAIT_STAGE(2);
    const unsigned char* vp = lds + L_VS + ((2 * pi * WAVES + w) * 32 + 8 * g) * 2;
    u32x4 vsA, vsB, vzA, vzB;
#pragma unroll
    for (int k = 0; k < 4; ++k) {
      const unsigned char* vq = vp + ((k ^ w) << 2);
      vsA[k] = *(const u32*)vq; vsB[k] = *(const u32*)(vq + WAVES * 64);
      vzA[k] = *(const u32*)(vq + TOK_WG * 2); vzB[k] = *(const u32*)(vq + TOK_WG * 2 + WAVES * 64);
    }
    const float2 qc = *(const float2*)(lds + L_QC + hh * 8);
    float mr = fmaxf(kA[0], kB[0]);
#pragma unroll
    for (int j = 1; j < 8; ++j) mr = fmaxf(mr, fmaxf(kA[j], kB[j]));
    mr = head_max(mr);
    float l = 0.f, wm = 0.f;
#pragma unroll
    for (int j = 0; j < 8; ++j) {
      const float pa = __builtin_amdgcn_exp2f((kA[j] - mr) * qc.x), pb = __builtin_amdgcn_exp2f((kB[j] - mr) * qc.x);
      l += pa + pb;
      kA[j] = pa * ((j & 1) ? bf_hi(vsA[j >> 1]) : bf_lo(vsA[j >> 1]));
      kB[j] = pb * ((j & 1) ? bf_hi(vsB[j >> 1]) : bf_lo(vsB[j >> 1]));
      wm = fmaxf(wm, fmaxf(fabsf(kA[j]), fabsf(kB[j])));
    }
    const int ew = __builtin_amdgcn_frexp_expf(head_max(wm));
    u32x4 PA, PB;
    float Z = 0.f;
#pragma unroll
    for (int k = 0; k < 4; ++k) {
      PA[k] = pkh(__builtin_amdgcn_ldexpf(kA[2 * k], 15 - ew), __builtin_amdgcn_ldexpf(kA[2 * k + 1], 15 - ew));
      PB[k] = pkh(__builtin_amdgcn_ldexpf(kB[2 * k], 15 - ew), __builtin_amdgcn_ldexpf(kB[2 * k + 1], 15 - ew));
      Z = fmaf(h_lo(PA[k]), bf_lo(vzA[k]), Z); Z = fmaf(h_hi(PA[k]), bf_hi(vzA[k]), Z);
      Z = fmaf(h_lo(PB[k]), bf_lo(vzB[k]), Z); Z = fmaf(h_hi(PB[k]), bf_hi(vzB[k]), Z);
    }
    l = head_sum(l); Z = head_sum(Z);
    if (lane < 16 && pi == 0)
      *(f32x4*)(lds + L_REC + (w * 4 + hh) * 16) = f32x4{fmaf(mr, qc.x, -qc.y), l, __builtin_amdgcn_ldexpf(Z, ew - 15), __int_as_float(ew + 9)};
    {
      const u32 one = 1u, ca = L_FLAG + 4;
      u64 sv;
      asm volatile("s_mov_b64 %0, exec\n\ts_mov_b64 exec, 1\n\tds_add_u32 %1, %2\n\ts_mov_b64 exec, %0" : "=&s"(sv) : "v"(ca), "v"(one) : "memory");
    }
    f32x4 acc[8];
#pragma unroll
    for (int k = 0; k < 8; ++k) acc[k] = f32x4{0.f, 0.f, 0.f, 0.f};
#pragma unroll 1
    for (int i = 0; i < 3; ++i) {
      const u32 so = wbase + (2 * i + 2) * TILE_STEP;
      const u32 mine = pi == i ? ~0u : 0u;
      v_tile<-1>(XA, PA, mine, acc, voff, rv, so, tw0, 32 * i + 160);
      v_tile<-1>(XB, PB, mine, acc, voff, rv, so + TILE_STEP, tw0, 32 * i + 176);
    }
    v_tile<16>(XA, PA, pi == 3 ? ~0u : 0u, acc, voff, rv, 0, tw0, 0);
    WAIT_STAGE(3);
    const float2 fac = *(const float2*)(lds + L_FAC + (w * 4 + g) * 8);
    u32x2 tag = *(const u32x2*)(lds + L_FLAG + 8);
    float pre[8];
#pragma unroll
    for (int k = 0; k < 8; ++k) pre[k] = fmaf((acc[k][0] + acc[k][1]) + acc[k][2], fac.x, -fac.y);
    u32 sto = WS_DAT + ((h * NSPLIT + w * 8 + (lane >> 3)) * NSPLIT + s) * 128 + (lane & 7) * 16;
    u32 la0 = L_ACC + ((w * 4 + g) * DH + 4 * r) * 4, la1 = L_ACC + (w * DH + 2 * lane) * 4;
    asm volatile("" : "+v"(pre[0]), "+v"(pre[1]), "+v"(pre[2]), "+v"(pre[3]), "+v"(pre[4]), "+v"(pre[5]), "+v"(pre[6]), "+v"(pre[7]),
                 "+v"(sto), "+v"(tag), "+v"(la0), "+v"(la1));
    __builtin_amdgcn_sched_barrier(0);
    v_tile<0>(XB, PB, pi == 3 ? ~0u : 0u, acc, voff, rv, 0, tw0, 0);
    {
      f32x4 o0, o1;
#pragma unroll
      for (int i = 0; i < 4; ++i) { o0[i] = fmaf(acc[i][3], fac.x, pre[i]); o1[i] = fmaf(acc[4 + i][3], fac.x, pre[4 + i]); }
      *(f32x4*)(lds + la0) = o0; *(f32x4*)(lds + la0 + 256) = o1;
    }
    __syncthreads();
    {
      float2 a[WAVES];
#pragma unroll
      for (int k = 0; k < WAVES; ++k) a[k] = *(const float2*)(lds + la1 + k * 4 * DH * 4);
      const u32x4 gd = {__float_as_uint((a[0].x + a[1].x) + (a[2].x + a[3].x)), tag.x, __float_as_uint((a[0].y + a[1].y) + (a[2].y + a[3].y)), tag.y};
      const __amdgpu_buffer_rsrc_t rw = __builtin_amdgcn_make_buffer_rsrc((void*)ws, 0, WS_END, 0x00020000);
      __builtin_amdgcn_raw_buffer_store_b128(gd, rw, sto, 0, SC1);
    }
  }
}

extern "C" void kernel_launch(void* const* d_in, const int* in_sizes, int n_in, void* d_out, int out_size,
                              void* d_ws, size_t ws_size, hipStream_t stream) {
  kivi_decode<<<HKV * NSPLIT, (WAVES + 1) * 64, 0, stream>>>((const uint16_t*)d_in[0], (const int*)d_in[1], (const int*)d_in[2],
      (const uint16_t*)d_in[3], (const uint16_t*)d_in[4], (const uint16_t*)d_in[5], (const uint16_t*)d_in[6], (unsigned char*)d_ws, (u32*)d_out);
}
```

```cpp
#include <hip/hip_runtime.h>
#include <hip/hip_bf16.h>
#include <stdint.h>

typedef unsigned u32;
typedef unsigned long long u64;
typedef u32 u32x4 __attribute__((ext_vector_type(4)));
typedef u32 u32x2 __attribute__((ext_vector_type(2)));
typedef float f32x4 __attribute__((ext_vector_type(4)));
typedef _Float16 f16x8 __attribute__((ext_vector_type(8)));
typedef _Float16 f16x2 __attribute__((ext_vector_type(2)));
typedef __bf16 bf16x2 __attribute__((ext_vector_type(2)));
typedef volatile __attribute__((address_space(3))) u32 lds_word;

constexpr int HQ = 32, HKV = 8, SKV = 32768, DH = 128, NGK = 2;
constexpr int NSPLIT = 32;
constexpr int WAVES = 4;
constexpr int TOK_WG = SKV / NSPLIT;
constexpr int TILE_ROWS = 4 * WAVES * 32;
constexpr int TILE_STEP = TILE_ROWS * 512;

constexpr int PACE_T = 3750, PACE_TO = 3720, PACE_A = 12;
constexpr int LATE = 285;
constexpr int STAT_LOOK = 2800;
constexpr int POLL_DELAY = 18;

constexpr int WS_EP = 0, WS_ML = 4096, WS_DAT = WS_ML + HKV * NSPLIT * 128, WS_END = WS_DAT + HKV * NSPLIT * NSPLIT * 128;
constexpr u32 L_ACC = 0;
constexpr u32 L_VS = L_ACC + WAVES * 4 * DH * 4;
constexpr u32 L_QB = L_VS + 2 * TOK_WG * 2;
constexpr u32 L_QC = L_QB + 64 * 16;
constexpr u32 L_REC = L_QC + 4 * 8;
constexpr u32 L_FAC = L_REC + WAVES * 4 * 16;
constexpr u32 L_FLAG = L_FAC + WAVES * 4 * 8;
constexpr u32 L_END = L_FLAG + 32;

constexpr u32 OOB = 0x80000000u;
constexpr u32 SPIN_CAP = 20000000u;
constexpr int SC1 = 16;

__device__ __forceinline__ float bf_lo(u32 x) { return __uint_as_float(x << 16); }
__device__ __forceinline__ float bf_hi(u32 x) { return __uint_as_float(x & 0xffff0000u); }
__device__ __forceinline__ u32 pk(u32 a, u32 b) { return a | (b << 16); }
template <int CTRL> __device__ __forceinline__ float dpp(float x) {
  return __int_as_float(__builtin_amdgcn_mov_dpp(__float_as_int(x), CTRL, 0xf, 0xf, true));
}
constexpr int XOR1 = 0xB1, XOR2 = 0x4E, HMIR = 0x141, ROR8 = 0x128;
__device__ __forceinline__ float rows_max(float x) {
  auto s = __builtin_amdgcn_permlane16_swap(__float_as_uint(x), __float_as_uint(x), false, false);
  x = fmaxf(__uint_as_float(s[0]), __uint_as_float(s[1]));
  auto t = __builtin_amdgcn_permlane32_swap(__float_as_uint(x), __float_as_uint(x), false, false);
  return fmaxf(__uint_as_float(t[0]), __uint_as_float(t[1]));
}
__device__ __forceinline__ float rows_sum(float x) {
  auto s = __builtin_amdgcn_permlane16_swap(__float_as_uint(x), __float_as_uint(x), false, false);
  x = __uint_as_float(s[0]) + __uint_as_float(s[1]);
  auto t = __builtin_amdgcn_permlane32_swap(__float_as_uint(x), __float_as_uint(x), false, false);
  return __uint_as_float(t[0]) + __uint_as_float(t[1]);
}
__device__ __forceinline__ float head_max(float x) { x = fmaxf(x, dpp<XOR1>(x)); x = fmaxf(x, dpp<XOR2>(x)); return rows_max(x); }
__device__ __forceinline__ float head_sum(float x) { x += dpp<XOR1>(x); x += dpp<XOR2>(x); return rows_sum(x); }
__device__ __forceinline__ float red8_sum(float x) { return rows_sum(x + dpp<ROR8>(x)); }
__device__ __forceinline__ float row_max16(float x) { x = fmaxf(x, dpp<XOR1>(x)); x = fmaxf(x, dpp<XOR2>(x)); x = fmaxf(x, dpp<HMIR>(x)); return fmaxf(x, dpp<ROR8>(x)); }
__device__ __forceinline__ float row_sum16(float x) { x += dpp<XOR1>(x); x += dpp<XOR2>(x); x += dpp<HMIR>(x); return x + dpp<ROR8>(x); }
__device__ __forceinline__ u32 pkh(float a, float b) { f16x2 h = {(_Float16)a, (_Float16)b}; return __builtin_bit_cast(u32, h); }
__device__ __forceinline__ float h_lo(u32 x) { return (float)__builtin_bit_cast(f16x2, x)[0]; }
__device__ __forceinline__ float h_hi(u32 x) { return (float)__builtin_bit_cast(f16x2, x)[1]; }
__device__ __forceinline__ f32x4 mfma(u32x4 a, u32x4 b, f32x4 c) {
  return __builtin_amdgcn_mfma_f32_16x16x32_f16(__builtin_bit_cast(f16x8, a), __builtin_bit_cast(f16x8, b), c, 0, 0, 0);
}
__device__ __forceinline__ u32x4 make_srd(const void* p, u32 bytes) {
  const u64 a = (u64)p;
  return u32x4{(u32)a, (u32)(a >> 32) & 0xffffu, bytes, 0x00020000u};
}
__device__ __forceinline__ u32 now() { return (u32)__builtin_amdgcn_s_memrealtime(); }

#define LD_TILE(C, reg, voff, srd, soff, n) \
  asm volatile("buffer_load_dwordx4 %0, %1, %2, %3 offen offset:%4 nt" : C(reg) : "v"((voff) ^ (((n) & 12) << 8)), "s"(srd), "s"(soff), "i"(((n) & 3) * 256) : "memory")
#define WAIT2(a, b, n) asm volatile("s_waitcnt vmcnt(%2)" : "+v"(a), "+v"(b) : "i"(n) : "memory")
#define WAIT4(a, b, c, d, n) asm volatile("s_waitcnt vmcnt(%4)" : "+v"(a), "+v"(b), "+v"(c), "+v"(d) : "i"(n) : "memory")
#define PACE(t0, n) \
  asm volatile("1:\n\ts_memrealtime vcc\n\ts_waitcnt lgkmcnt(0)\n\ts_sub_i32 vcc_lo, vcc_lo, %0\n\ts_cmp_ge_i32 vcc_lo, 0\n\ts_cbranch_scc1 2f\n\ts_sleep 1\n\ts_branch 1b\n2:" \
               :: "s"((t0).x + (u32)((((int)(n) - PACE_A) * (int)(t0).y) >> 8)) : "scc", "vcc", "memory")
#define WAIT_STAGE(stage) do { u32 v_, s_; const u32 a_ = L_FLAG; \
  asm volatile("1:\n\tds_read_b32 %0, %2\n\ts_waitcnt lgkmcnt(0)\n\tv_readfirstlane_b32 %1, %0\n\ts_cmp_ge_u32 %1, %3\n\ts_cbranch_scc1 2f\n\ts_sleep 1\n\ts_branch 1b\n2:" \
               : "=&v"(v_), "=&s"(s_) : "v"(a_), "i"(stage) : "scc", "memory"); } while (0)

__device__ __forceinline__ void k_keep(float& keep, const f32x4 Dm, u32 mine) {
  const float S = ((dpp<0x00>(Dm[0]) + dpp<0x55>(Dm[1])) + dpp<0xAA>(Dm[2])) + dpp<0xFF>(Dm[3]);
  keep = __uint_as_float((__float_as_uint(S) & mine) | (__float_as_uint(keep) & ~mine));
}
template <int REFILL>
__device__ __forceinline__ void k_tile(u32x4 (&X)[16], const u32x4 QB, float (&keep)[8], u32 mine, u32 voff, u32x4 srd, u32 soff, u32x2 t0, int n0) {
  f32x4 Dp;
#pragma unroll
  for (int j = 0; j < 8; ++j) {
    WAIT2(X[2 * j], X[2 * j + 1], 2 * j <= REFILL ? 30 : 30 + REFILL - 2 * j);
    const u32x4 x = X[2 * j], y = X[2 * j + 1];
    const u32x4 A = {pk(x.x, x.y), pk(x.z, x.w), pk(y.x, y.y), pk(y.z, y.w)};
    if (2 * j < REFILL) {
      if ((j & 1) == 0) PACE(t0, n0 + 2 * j);
      LD_TILE("+v", X[2 * j], voff, srd, soff, 2 * j);
      LD_TILE("+v", X[2 * j + 1], voff, srd, soff, 2 * j + 1);
    }
    const f32x4 Dm = mfma(A, QB, f32x4{0.f, 0.f, 0.f, 0.f});
    __builtin_amdgcn_sched_barrier(0);
    if (j > 0) k_keep(keep[j - 1], Dp, mine);
    __builtin_amdgcn_sched_barrier(0);
    Dp = Dm;
  }
  k_keep(keep[7], Dp, mine);
}
template <int LEFT>
__device__ __forceinline__ void v_tile(u32x4 (&X)[16], const u32x4 PW, u32 mine, f32x4 (&acc)[8], u32 voff, u32x4 srd, u32 soff, u32x2 t0, int n0) {
  u32x4 Bv[8];
  const u32x4 A = {PW.x & mine, PW.y & mine, PW.z & mine, PW.w & mine};
#pragma unroll
  for (int u = 0; u < 4; ++u) {
    WAIT4(X[4 * u], X[4 * u + 1], X[4 * u + 2], X[4 * u + 3], LEFT < 0 ? 28 : LEFT + 12 - 4 * u);
#pragma unroll
    for (int hf = 0; hf < 2; ++hf)
#pragma unroll
      for (int i = 0; i < 4; ++i) Bv[hf * 4 + i][u] = pk(X[4 * u + hf][i], X[4 * u + 2 + hf][i]);
    if (LEFT < 0) {
      PACE(t0, n0 + 4 * u);
#pragma unroll
      for (int n = 4 * u; n < 4 * u + 4; ++n) LD_TILE("+v", X[n], voff, srd, soff, n);
    }
    __builtin_amdgcn_sched_barrier(0);
  }
#pragma unroll
  for (int k = 0; k < 8; ++k) acc[k] = mfma(A, Bv[k], acc[k]);
  __builtin_amdgcn_sched_barrier(0);
}

__global__ void __launch_bounds__((WAVES + 1) * 64, 2)
kivi_decode(const uint16_t* __restrict__ q, const int* __restrict__ qk, const int* __restrict__ qv,
            const uint16_t* __restrict__ ks, const uint16_t* __restrict__ kz,
            const uint16_t* __restrict__ vs, const uint16_t* __restrict__ vz,
            unsigned char* ws, u32* __restrict__ out) {
  __shared__ __attribute__((aligned(16))) unsigned char lds[L_END];
  u32x2 tw0 = {now(), PACE_T};
  asm volatile("" :: "s"(q), "s"(qk), "s"(qv), "s"(ks), "s"(kz), "s"(vs), "s"(vz), "s"(ws), "s"(out));
  const int b = blockIdx.x, h = b >> 5;
  const int s = ((b & 3) * 2 + ((b >> 4) & 1)) * 4 + ((b >> 2) & 1) * 2 + ((b >> 3) & 1);
  const int tid = threadIdx.x, lane = tid & 63, w = __builtin_amdgcn_readfirstlane(tid >> 6);
  const int g = lane >> 4, r = lane & 15, hh = r >> 2, pi = r & 3;
  const int tok0 = h * SKV + (s >> 2) * (4 * TOK_WG) + (s & 3) * (WAVES * 32);

  if (w == WAVES) {
    lds_word* flag = (lds_word*)(lds + L_FLAG);
    __builtin_amdgcn_s_setprio(3);
    if (lane < 2) flag[lane] = 0u;
    const int d0 = 16 * g + 4 * pi;
    const uint16_t* qp = q + (h * 4 + hh) * DH + d0;
    const uint16_t* ksp = ks + (h * NGK + (s >> 4)) * DH + d0;
    const uint16_t* kzp = kz + (h * NGK + (s >> 4)) * DH + d0;
    const u32x2 q0 = *(const u32x2*)qp, q1 = *(const u32x2*)(qp + 64), s0 = *(const u32x2*)ksp, s1 = *(const u32x2*)(ksp + 64);
    const u32x2 z0 = *(const u32x2*)kzp, z1 = *(const u32x2*)(kzp + 64);
    asm volatile("s_waitcnt lgkmcnt(0)\n\ts_barrier" ::: "memory");
    {
      const float qq[8] = {bf_lo(q0.x) * bf_lo(s0.x), bf_hi(q0.x) * bf_hi(s0.x), bf_lo(q0.y) * bf_lo(s0.y), bf_hi(q0.y) * bf_hi(s0.y),
                           bf_lo(q1.x) * bf_lo(s1.x), bf_hi(q1.x) * bf_hi(s1.x), bf_lo(q1.y) * bf_lo(s1.y), bf_hi(q1.y) * bf_hi(s1.y)};
      const float zz[8] = {bf_lo(z0.x), bf_hi(z0.x), bf_lo(z0.y), bf_hi(z0.y), bf_lo(z1.x), bf_hi(z1.x), bf_lo(z1.y), bf_hi(z1.y)};
      float qm = 0.f;
#pragma unroll
      for (int k = 0; k < 8; ++k) qm = fmaxf(qm, fabsf(qq[k]));
      const int eq = __builtin_amdgcn_frexp_expf(head_max(qm));
      u32x4 QB;
#pragma unroll
      for (int k = 0; k < 4; ++k) QB[k] = pkh(__builtin_amdgcn_ldexpf(qq[2 * k], 15 - eq), __builtin_amdgcn_ldexpf(qq[2 * k + 1], 15 - eq));
      *(u32x4*)(lds + L_QB + lane * 16) = QB;
      if (lane == 0) flag[4] = now() - LATE;
      asm volatile("s_waitcnt lgkmcnt(0)" ::: "memory");
      if (lane == 0) flag[0] = 1u;
      float cq = 0.f;
#pragma unroll
      for (int k = 0; k < 4; ++k) { cq = fmaf(h_lo(QB[k]), zz[2 * k], cq); cq = fmaf(h_hi(QB[k]), zz[2 * k + 1], cq); }
      cq = head_sum(cq);
      constexpr float SC = 0.088388347648318440f * 1.4426950408889634f;
      if (lane < 16 && pi == 0) *(float2*)(lds + L_QC + hh * 8) = make_float2(__builtin_amdgcn_ldexpf(SC, eq + 9), __builtin_amdgcn_ldexpf(cq, eq - 15) * SC);
    }
    u64* epp = (u64*)(ws + WS_EP + h * 256);
    const u64 ep = __hip_atomic_load(epp, __ATOMIC_RELAXED, __HIP_MEMORY_SCOPE_AGENT);
    {
      u32x4 t[4];
#pragma unroll
      for (int k = 0; k < 4; ++k) t[k] = __builtin_nontemporal_load((const u32x4*)((k < 2 ? vs : vz) + tok0 + ((k & 1) * 4 + g) * TILE_ROWS + r * 8));
#pragma unroll
      for (int k = 0; k < 4; ++k) *(u32x4*)(lds + L_VS + k * 1024 + lane * 16) = t[k];
    }
    const u32 tagA = __builtin_amdgcn_readfirstlane((u32)ep) + 1u, tagB = __builtin_amdgcn_readfirstlane((u32)(ep >> 32)) + 1u;
    if (lane == 0) { flag[2] = tagA; flag[3] = tagB; }
    asm volatile("s_waitcnt lgkmcnt(0)" ::: "memory");
    if (lane == 0) flag[0] = 2u;
    while (flag[1] < WAVES) __builtin_amdgcn_s_sleep(8);
    asm volatile("" ::: "memory");
    const __amdgpu_buffer_rsrc_t rw = __builtin_amdgcn_make_buffer_rsrc((void*)ws, 0, WS_END, 0x00020000);
    const float* rec = (const float*)(lds + L_REC);
    {
      float M = -INFINITY, L = 0.f;
#pragma unroll
      for (int k = 0; k < WAVES; ++k) M = fmaxf(M, rec[(k * 4 + g) * 4]);
#pragma unroll
      for (int k = 0; k < WAVES; ++k) L = fmaf(__builtin_amdgcn_exp2f(rec[(k * 4 + g) * 4] - M), rec[(k * 4 + g) * 4 + 1], L);
      const u32x4 gm = {__float_as_uint(M), tagA, __float_as_uint(L), tagB};
      __builtin_amdgcn_raw_buffer_store_b128(gm, rw, r == 0 ? (u32)(WS_ML + (h * NSPLIT + s) * 128 + g * 16) : OOB, 0, SC1);
    }
    while ((int)(now() - tw0.x) < STAT_LOOK) __builtin_amdgcn_s_sleep(8);
    {
      const u32 mo = WS_ML + (h * NSPLIT + r) * 128 + g * 16, tstart = now();
      u32x4 g0, g1;
      bool ok0 = false, ok1 = false;
      for (;;) {
        const u32x4 x0 = __builtin_amdgcn_raw_buffer_load_b128(rw, ok0 ? OOB : mo, 0, SC1);
        const u32x4 x1 = __builtin_amdgcn_raw_buffer_load_b128(rw, ok1 ? OOB : mo + 16 * 128, 0, SC1);
        if (!ok0) { g0 = x0; ok0 = (x0.y == tagA) & (x0.w == tagB); }
        if (!ok1) { g1 = x1; ok1 = (x1.y == tagA) & (x1.w == tagB); }
        if (__all(ok0 & ok1) || now() - tstart > SPIN_CAP) break;
        __builtin_amdgcn_s_sleep(32);
        asm volatile("" ::: "memory");
      }
      const float m0 = __uint_as_float(g0.x), m1 = __uint_as_float(g1.x);
      const float M = row_max16(fmaxf(m0, m1));
      const float L = row_sum16(fmaf(__builtin_amdgcn_exp2f(m0 - M), __uint_as_float(g0.z), __builtin_amdgcn_exp2f(m1 - M) * __uint_as_float(g1.z)));
      if (r < WAVES) {
        const f32x4 rc = *(const f32x4*)(rec + (r * 4 + g) * 4);
        const float f = __builtin_amdgcn_exp2f(rc[0] - M) / L;
        *(float2*)(lds + L_FAC + (r * 4 + g) * 8) = make_float2(__builtin_amdgcn_ldexpf(f, __float_as_int(rc[3])), f * rc[2]);
      }
      asm volatile("s_waitcnt lgkmcnt(0)" ::: "memory");
      if (lane == 0) flag[0] = 3u;
    }
    const u32 doff = WS_DAT + ((h * NSPLIT + s) * NSPLIT + (lane >> 3)) * 128 + (lane & 7) * 16;
    const u32x4 rws = make_srd(ws, WS_END);
    const __amdgpu_buffer_rsrc_t ro = __builtin_amdgcn_make_buffer_rsrc((void*)out, 0, HQ * DH * 2, 0x00020000);
    u32 oo = lane < 8 ? (u32)((((h * 4 + (s >> 3)) * DH + (s & 7) * 16) / 2 + lane) * 4) : OOB;
    asm volatile("" : "+v"(oo));
    float p0, p1;
    u32x4 ring[12];
    {
      u64 m0, m1, m2, m3, mt;
      u32 t0_;
#define POLL_TAKE(m, x, y, z, w_, d0_, d1_) \
  "v_cmp_eq_u32 vcc, %22, v" #y "\n\tv_cmp_eq_u32 %6, %23, v" #w_ "\n\ts_and_b64 exec, vcc, %6\n\t" \
  "v_mov_b32 v" #d0_ ", v" #x "\n\tv_mov_b32 v" #d1_ ", v" #z "\n\ts_andn2_b64 %" #m ", %" #m ", exec\n\ts_mov_b64 exec, -1\n\t"
      asm volatile(
          "s_not_b32 %7, %22\n\t"
          "v_mov_b32 v65, %7\n\tv_mov_b32 v69, %7\n\tv_mov_b32 v73, %7\n\tv_mov_b32 v77, %7\n\t"
          "v_mov_b32 v81, %7\n\tv_mov_b32 v85, %7\n\tv_mov_b32 v89, %7\n\tv_mov_b32 v93, %7\n\t"
          "v_mov_b32 v97, %7\n\tv_mov_b32 v101, %7\n\tv_mov_b32 v105, %7\n\tv_mov_b32 v109, %7\n\t"
          "s_mov_b64 %2, -1\n\ts_mov_b64 %3, -1\n\ts_mov_b64 %4, -1\n\ts_mov_b64 %5, -1\n\t"
          "s_memrealtime vcc\n\ts_waitcnt lgkmcnt(0)\n\ts_mov_b32 %7, vcc_lo\n\t"
          "s_barrier\n\ts_sleep %c25\n"
          "1:\n\t"
          "s_or_b64 exec, %2, 1\n\tbuffer_load_dwordx4 v[64:67], %20, %21, 0 offen sc1\n\t"
          "s_or_b64 exec, %3, 1\n\tbuffer_load_dwordx4 v[68:71], %20, %21, 0 offen offset:1024 sc1\n\t"
          "s_or_b64 exec, %4, 1\n\tbuffer_load_dwordx4 v[72:75], %20, %21, 0 offen offset:2048 sc1\n\t"
          "s_or_b64 exec, %5, 1\n\tbuffer_load_dwordx4 v[76:79], %20, %21, 0 offen offset:3072 sc1\n\t"
          "s_mov_b64 exec, -1\n\ts_waitcnt vmcnt(8)\n\t"
          POLL_TAKE(2, 80, 81, 82, 83, 112, 113)
          POLL_TAKE(3, 84, 85, 86, 87, 114, 115)
          POLL_TAKE(4, 88, 89, 90, 91, 116, 117)
          POLL_TAKE(5, 92, 93, 94, 95, 118, 119)
          "s_or_b64 vcc, %2, %3\n\ts_or_b64 vcc, vcc, %4\n\ts_or_b64 vcc, vcc, %5\n\ts_cbranch_scc0 2f\n\t"
          "s_or_b64 exec, %2, 1\n\tbuffer_load_dwordx4 v[80:83], %20, %21, 0 offen sc1\n\t"
          "s_or_b64 exec, %3, 1\n\tbuffer_load_dwordx4 v[84:87], %20, %21, 0 offen offset:1024 sc1\n\t"
          "s_or_b64 exec, %4, 1\n\tbuffer_load_dwordx4 v[88:91], %20, %21, 0 offen offset:2048 sc1\n\t"
          "s_or_b64 exec, %5, 1\n\tbuffer_load_dwordx4 v[92:95], %20, %21, 0 offen offset:3072 sc1\n\t"
          "s_mov_b64 exec, -1\n\ts_waitcnt vmcnt(8)\n\t"
          POLL_TAKE(2, 96, 97, 98, 99, 112, 113)
          POLL_TAKE(3, 100, 101, 102, 103, 114, 115)
          POLL_TAKE(4, 104, 105, 106, 107, 116, 117)
          POLL_TAKE(5, 108, 109, 110, 111, 118, 119)
          "s_or_b64 vcc, %2, %3\n\ts_or_b64 vcc, vcc, %4\n\ts_or_b64 vcc, vcc, %5\n\ts_cbranch_scc0 2f\n\t"
          "s_or_b64 exec, %2, 1\n\tbuffer_load_dwordx4 v[96:99], %20, %21, 0 offen sc1\n\t"
          "s_or_b64 exec, %3, 1\n\tbuffer_load_dwordx4 v[100:103], %20, %21, 0 offen offset:1024 sc1\n\t"
          "s_or_b64 exec, %4, 1\n\tbuffer_load_dwordx4 v[104:107], %20, %21, 0 offen offset:2048 sc1\n\t"
          "s_or_b64 exec, %5, 1\n\tbuffer_load_dwordx4 v[108:111], %20, %21, 0 offen offset:3072 sc1\n\t"
          "s_mov_b64 exec, -1\n\ts_waitcnt vmcnt(8)\n\t"
          POLL_TAKE(2, 64, 65, 66, 67, 112, 113)
          POLL_TAKE(3, 68, 69, 70, 71, 114, 115)
          POLL_TAKE(4, 72, 73, 74, 75, 116, 117)
          POLL_TAKE(5, 76, 77, 78, 79, 118, 119)
          "s_or_b64 vcc, %2, %3\n\ts_or_b64 vcc, vcc, %4\n\ts_or_b64 vcc, vcc, %5\n\ts_cbranch_scc0 2f\n\t"
          "s_memrealtime vcc\n\ts_waitcnt lgkmcnt(0)\n\ts_sub_u32 vcc_lo, vcc_lo, %7\n\ts_cmp_lt_u32 vcc_lo, %24\n\t"
          "s_cbranch_scc1 1b\n"
          "2:\n\t"
          "v_add_f32 %0, v112, v114\n\tv_add_f32 %1, v113, v115\n\tv_add_f32 v116, v116, v118\n\tv_add_f32 v117, v117, v119\n\t"
          "v_add_f32 %0, %0, v116\n\tv_add_f32 %1, %1, v117"
          : "=&v"(p0), "=&v"(p1), "=&s"(m0), "=&s"(m1), "=&s"(m2), "=&s"(m3), "=&s"(mt), "=&s"(t0_),
            "=&{v[64:67]}"(ring[0]), "=&{v[68:71]}"(ring[1]), "=&{v[72:75]}"(ring[2]), "=&{v[76:79]}"(ring[3]), "=&{v[80:83]}"(ring[4]),
            "=&{v[84:87]}"(ring[5]), "=&{v[88:91]}"(ring[6]), "=&{v[92:95]}"(ring[7]), "=&{v[96:99]}"(ring[8]), "=&{v[100:103]}"(ring[9]),
            "=&{v[104:107]}"(ring[10]), "=&{v[108:111]}"(ring[11])
          : "v"(doff), "s"(rws), "s"(tagA), "s"(tagB), "s"(SPIN_CAP), "i"(POLL_DELAY)
          : "vcc", "scc", "memory", "v112", "v113", "v114", "v115", "v116", "v117", "v118", "v119");
    }
    const bf16x2 o = {(__bf16)red8_sum(p0), (__bf16)red8_sum(p1)};
    __builtin_amdgcn_raw_buffer_store_b32(__builtin_bit_cast(u32, o), ro, oo, 0, 0);
    if (s == 0 && lane == 0) __hip_atomic_store(epp, ((u64)tagB << 32) | tagA, __ATOMIC_RELAXED, __HIP_MEMORY_SCOPE_AGENT);
    asm volatile("s_waitcnt vmcnt(0)"
                 :: "{v[64:67]}"(ring[0]), "{v[68:71]}"(ring[1]), "{v[72:75]}"(ring[2]), "{v[76:79]}"(ring[3]), "{v[80:83]}"(ring[4]),
                 "{v[84:87]}"(ring[5]), "{v[88:91]}"(ring[6]), "{v[92:95]}"(ring[7]), "{v[96:99]}"(ring[8]), "{v[100:103]}"(ring[9]),
                 "{v[104:107]}"(ring[10]), "{v[108:111]}"(ring[11]) : "memory");
  } else {
    { u32 xcc; asm volatile("s_getreg_b32 %0, hwreg(HW_REG_XCC_ID)" : "=s"(xcc)); tw0.y = (xcc & 1) ? PACE_TO : PACE_T; }
    const u32x4 rk = make_srd(qk, HKV * SKV * DH * 4), rv = make_srd(qv, HKV * SKV * DH * 4);
    const u32 koff = (w << 10) + hh * 4096 + g * 64 + pi * 16;
    const u32 voff = (w << 10) + g * 4096 + r * 16;
    const u32 wbase = (u32)(tok0 + w * 32) * 512u;
    u32x4 XA[16], XB[16];
#pragma unroll
    for (int n = 0; n < 16; ++n) {
      if (n == 12) __builtin_amdgcn_s_barrier();
      LD_TILE("=v", XA[n], koff, rk, wbase, n);
    }
#pragma unroll
    for (int n = 0; n < 16; ++n) { if (n % 4 == 0) PACE(tw0, n + 16); LD_TILE("=v", XB[n], koff, rk, wbase + TILE_STEP, n); }
    WAIT_STAGE(1);
    { const u32 org = __builtin_amdgcn_readfirstlane(*(const u32*)(lds + L_FLAG + 16)); tw0.x = (int)(org - tw0.x) > 0 ? org : tw0.x; }
    const u32x4 QB = *(const u32x4*)(lds + L_QB + lane * 16);

    float kA[8], kB[8];
#pragma unroll
    for (int j = 0; j < 8; ++j) { kA[j] = 0.f; kB[j] = 0.f; }
#pragma unroll 1
    for (int i = 0; i < 3; ++i) {
      const u32 so = wbase + (2 * i + 2) * TILE_STEP;
      const u32 mine = pi == i ? ~0u : 0u;
      k_tile<16>(XA, QB, kA, mine, koff, rk, so, tw0, 32 * i + 32);
      k_tile<16>(XB, QB, kB, mine, koff, rk, so + TILE_STEP, tw0, 32 * i + 48);
    }
    k_tile<16>(XA, QB, kA, pi == 3 ? ~0u : 0u, voff, rv, wbase, tw0, 128);
    k_tile<8>(XB, QB, kB, pi == 3 ? ~0u : 0u, voff, rv, wbase + TILE_STEP, tw0, 144);
#define REFILL4(n0_) do { __builtin_amdgcn_sched_barrier(0); PACE(tw0, 144 + (n0_)); \
      _Pragma("unroll") for (int n = (n0_); n < (n0_) + 4; ++n) LD_TILE("=v", XB[n], voff, rv, wbase + TILE_STEP, n); __builtin_amdgcn_sched_barrier(0); } while (0)
    WAIT_STAGE(2);
    const unsigned char* vp = lds + L_VS + ((2 * pi * WAVES + w) * 32 + 8 * g) * 2;
    u32x4 vsA, vsB, vzA, vzB;
#pragma unroll
    for (int k = 0; k < 4; ++k) {
      const unsigned char* vq = vp + ((k ^ w) << 2);
      vsA[k] = *(const u32*)vq; vsB[k] = *(const u32*)(vq + WAVES * 64);
      vzA[k] = *(const u32*)(vq + TOK_WG * 2); vzB[k] = *(const u32*)(vq + TOK_WG * 2 + WAVES * 64);
    }
    const float2 qc = *(const float2*)(lds + L_QC + hh * 8);
    float mr = fmaxf(kA[0], kB[0]);
#pragma unroll
    for (int j = 1; j < 8; ++j) mr = fmaxf(mr, fmaxf(kA[j], kB[j]));
    mr = head_max(mr);
    float l = 0.f, wm = 0.f;
#pragma unroll
    for (int j = 0; j < 8; ++j) {
      const float pa = __builtin_amdgcn_exp2f((kA[j] - mr) * qc.x), pb = __builtin_amdgcn_exp2f((kB[j] - mr) * qc.x);
      l += pa + pb;
      kA[j] = pa * ((j & 1) ? bf_hi(vsA[j >> 1]) : bf_lo(vsA[j >> 1]));
      kB[j] = pb * ((j & 1) ? bf_hi(vsB[j >> 1]) : bf_lo(vsB[j >> 1]));
      wm = fmaxf(wm, fmaxf(fabsf(kA[j]), fabsf(kB[j])));
    }
    int ew = __builtin_amdgcn_frexp_expf(head_max(wm));
    asm volatile("" : "+v"(ew), "+v"(l));
    REFILL4(8);
    u32x4 PA, PB;
    float Z = 0.f;
#pragma unroll
    for (int k = 0; k < 4; ++k) {
      PA[k] = pkh(__builtin_amdgcn_ldexpf(kA[2 * k], 15 - ew), __builtin_amdgcn_ldexpf(kA[2 * k + 1], 15 - ew));
      PB[k] = pkh(__builtin_amdgcn_ldexpf(kB[2 * k], 15 - ew), __builtin_amdgcn_ldexpf(kB[2 * k + 1], 15 - ew));
      Z = fmaf(h_lo(PA[k]), bf_lo(vzA[k]), Z); Z = fmaf(h_hi(PA[k]), bf_hi(vzA[k]), Z);
      Z = fmaf(h_lo(PB[k]), bf_lo(vzB[k]), Z); Z = fmaf(h_hi(PB[k]), bf_hi(vzB[k]), Z);
    }
    l = head_sum(l); Z = head_sum(Z);
    if (lane < 16 && pi == 0)
      *(f32x4*)(lds + L_REC + (w * 4 + hh) * 16) = f32x4{fmaf(mr, qc.x, -qc.y), l, __builtin_amdgcn_ldexpf(Z, ew - 15), __int_as_float(ew + 9)};
    {
      const u32 one = 1u, ca = L_FLAG + 4;
      u64 sv;
      asm volatile("s_mov_b64 %0, exec\n\ts_mov_b64 exec, 1\n\tds_add_u32 %1, %2\n\ts_mov_b64 exec, %0" : "=&s"(sv) : "v"(ca), "v"(one) : "memory");
    }
    REFILL4(12);
    f32x4 acc[8];
#pragma unroll
    for (int k = 0; k < 8; ++k) acc[k] = f32x4{0.f, 0.f, 0.f, 0.f};
#pragma unroll 1
    for (int i = 0; i < 3; ++i) {
      const u32 so = wbase + (2 * i + 2) * TILE_STEP;
      const u32 mine = pi == i ? ~0u : 0u;
      v_tile<-1>(XA, PA, mine, acc, voff, rv, so, tw0, 32 * i + 160);
      v_tile<-1>(XB, PB, mine, acc, voff, rv, so + TILE_STEP, tw0, 32 * i + 176);
    }
    v_tile<16>(XA, PA, pi == 3 ? ~0u : 0u, acc, voff, rv, 0, tw0, 0);
    WAIT_STAGE(3);
    const float2 fac = *(const float2*)(lds + L_FAC + (w * 4 + g) * 8);
    u32x2 tag = *(const u32x2*)(lds + L_FLAG + 8);
    float pre[8];
#pragma unroll
    for (int k = 0; k < 8; ++k) pre[k] = fmaf((acc[k][0] + acc[k][1]) + acc[k][2], fac.x, -fac.y);
    u32 sto = WS_DAT + ((h * NSPLIT + w * 8 + (lane >> 3)) * NSPLIT + s) * 128 + (lane & 7) * 16;
    u32 la0 = L_ACC + ((w * 4 + g) * DH + 4 * r) * 4, la1 = L_ACC + (w * DH + 2 * lane) * 4;
    asm volatile("" : "+v"(pre[0]), "+v"(pre[1]), "+v"(pre[2]), "+v"(pre[3]), "+v"(pre[4]), "+v"(pre[5]), "+v"(pre[6]), "+v"(pre[7]),
                 "+v"(sto), "+v"(tag), "+v"(la0), "+v"(la1));
    __builtin_amdgcn_sched_barrier(0);
    v_tile<0>(XB, PB, pi == 3 ? ~0u : 0u, acc, voff, rv, 0, tw0, 0);
    {
      f32x4 o0, o1;
#pragma unroll
      for (int i = 0; i < 4; ++i) { o0[i] = fmaf(acc[i][3], fac.x, pre[i]); o1[i] = fmaf(acc[4 + i][3], fac.x, pre[4 + i]); }
      *(f32x4*)(lds + la0) = o0; *(f32x4*)(lds + la0 + 256) = o1;
    }
    __syncthreads();
    {
      float2 a[WAVES];
#pragma unroll
      for (int k = 0; k < WAVES; ++k) a[k] = *(const float2*)(lds + la1 + k * 4 * DH * 4);
      const u32x4 gd = {__float_as_uint((a[0].x + a[1].x) + (a[2].x + a[3].x)), tag.x, __float_as_uint((a[0].y + a[1].y) + (a[2].y + a[3].y)), tag.y};
      const __amdgpu_buffer_rsrc_t rw = __builtin_amdgcn_make_buffer_rsrc((void*)ws, 0, WS_END, 0x00020000);
      __builtin_amdgcn_raw_buffer_store_b128(gd, rw, sto, 0, SC1);
    }
  }
}

extern "C" void kernel_launch(void* const* d_in, const int* in_sizes, int n_in, void* d_out, int out_size,
                              void* d_ws, size_t ws_size, hipStream_t stream) {
  kivi_decode<<<HKV * NSPLIT, (WAVES + 1) * 64, 0, stream>>>((const uint16_t*)d_in[0], (const int*)d_in[1], (const int*)d_in[2],
      (const uint16_t*)d_in[3], (const uint16_t*)d_in[4], (const uint16_t*)d_in[5], (const uint16_t*)d_in[6], (unsigned char*)d_ws, (u32*)d_out);
}
```
